```python
import torch

def _softmax(x, axis=-1):
    return torch.softmax(x, dim=axis)

def _gelu(x):
    return torch.nn.functional.gelu(x, approximate='tanh')

def _silu(x):
    return torch.nn.functional.silu(x)

def _sigmoid(x):
    return torch.sigmoid(x)

def _layernorm(x, axis=-1, eps=1e-05):
    m = x.mean(dim=axis, keepdim=True)
    v = x.var(dim=axis, keepdim=True, unbiased=False)
    return (x - m) / torch.sqrt(v + eps)

def _scatter_add(tgt, idx, src, axis=0):
    return tgt.clone().index_add_(axis, idx, src)

def _segment_sum(src, seg, n):
    out = torch.zeros((n,) + src.shape[1:], dtype=src.dtype)
    return out.index_add_(0, seg, src)

def _pad(x, pw):
    flat = tuple((v for pair in reversed(pw) for v in pair))
    return torch.nn.functional.pad(x, flat)
kernel_name = 'adaLN_cond__sliding_block_cached__dense_activation_silu__decode'

def setup_inputs():

    _w = lambda a: (lambda t: t.to(torch.bfloat16) if t.is_floating_point() else t)(torch.as_tensor(a))

    def _A_setup():
        B, Sq, D = (2, 1, 448)
        Hq, Hkv, Sk, Dh = (16, 8, 8192, 256)
        assert Hq % Hkv == 0, (Hq, Hkv)
        return {'x': _w((lambda *s: torch.randn(*s, dtype=torch.bfloat16) * 0.1)(B, Sq, D)), 'k_cache': _w((lambda *s: torch.randn(*s, dtype=torch.bfloat16) * 0.8)(B, Hkv, Sk, Dh)), 'v_cache': _w((lambda *s: torch.randn(*s, dtype=torch.bfloat16) * 3.2)(B, Hkv, Sk, Dh)), 'wq': _w((lambda *s: torch.randn(*s, dtype=torch.bfloat16) * 0.8)(D, Hq * Dh) * D ** (-0.5)), 'wo': _w((lambda *s: torch.randn(*s, dtype=torch.bfloat16) * 3.2)(Hq * Dh, D) * (Hq * Dh) ** (-0.5))}
    _A_in = _A_setup()

    def _B_setup():
        B, S, D, F = (2, 1, 448, 1792)
        return {'x': _w((lambda *s: torch.randn(*s, dtype=torch.bfloat16) * 0.1)(B, S, D)), 'w1': _w((lambda *s: torch.randn(*s, dtype=torch.bfloat16) * 3.2)(D, F) * D ** (-0.5)), 'w2': _w((lambda *s: torch.randn(*s, dtype=torch.bfloat16) * 3.2)(F, D) * F ** (-0.5))}
    _B_in = _B_setup()
    return {'A_x': _A_in['x'], 'A_k_cache': _A_in['k_cache'], 'A_v_cache': _A_in['v_cache'], 'A_wq': _A_in['wq'], 'A_wo': _A_in['wo'], 'B_x': _B_in['x'], 'B_w1': _B_in['w1'], 'B_w2': _B_in['w2'], 'c': _w((lambda *s: torch.randn(*s, dtype=torch.bfloat16) * 0.2)(2, 448) * 8.0), 'w_mod': _w((lambda *s: torch.randn(*s, dtype=torch.bfloat16) * 0.2)(448, 6 * 448) * 448 ** (-0.5) * 8.0)}

def reference(A_x, A_k_cache, A_v_cache, A_wq, A_wo, B_x, B_w1, B_w2, c, w_mod):

    def _A_ref(x, k_cache, v_cache, wq, wo):
        B, Sq, D = x.shape
        Hq, Dh = (16, 256)
        Hkv, Sk = (k_cache.shape[1], k_cache.shape[2])
        W = min(4096, max(256, Sk // 4))
        rep = Hq // Hkv
        q = (lambda x, *d: x.permute(*d))((x @ wq).reshape(B, Sq, Hq, Dh), 0, 2, 1, 3)
        k = (lambda x, n, axis: torch.repeat_interleave(x, n, dim=axis))(k_cache[:, :, Sk - W:, :], rep, axis=1)
        v = (lambda x, n, axis: torch.repeat_interleave(x, n, dim=axis))(v_cache[:, :, Sk - W:, :], rep, axis=1)
        scores = torch.einsum('bhqd,bhkd->bhqk', q, k) * Dh ** (-0.5)
        p = _softmax(scores, axis=-1)
        o = torch.einsum('bhqk,bhkd->bhqd', p, v)
        return (lambda x, *d: x.permute(*d))(o, 0, 2, 1, 3).reshape(B, Sq, Hq * Dh) @ wo

    def _B_ref(x, w1, w2):
        h = torch.einsum('bsd,df->bsf', x, w1)
        h = _silu(h)
        out = torch.einsum('bsf,fd->bsd', h, w2)
        return out
    x = A_x
    mod = (_silu(c) @ w_mod).reshape(c.shape[0], 6, 1, -1)
    sa, ca, ga, sb, cb, gb = (mod[:, 0].clone(), mod[:, 1].clone(), mod[:, 2].clone(), mod[:, 3].clone(), mod[:, 4].clone(), mod[:, 5].clone())
    h = _layernorm(x, axis=-1) * (1 + ca) + sa
    x = x + ga * _A_ref(h, A_k_cache, A_v_cache, A_wq, A_wo)
    h = _layernorm(x, axis=-1) * (1 + cb) + sb
    x = x + gb * _B_ref(h, B_w1, B_w2)
    return x
```

```cpp
#include <hip/hip_runtime.h>
#include <hip/hip_bf16.h>
#include <stdint.h>

typedef unsigned short u16;
constexpr int B = 2, D = 448, HQ = 16, HKV = 8, SK = 8192, DH = 256, W = 2048, F = 1792;
constexpr int NSPLIT = 16, KPS = W / NSPLIT;

__device__ __forceinline__ float bf2f(u16 v) { return __uint_as_float((unsigned)v << 16); }
__device__ __forceinline__ float rbf(float f) { __hip_bfloat16 b = __float2bfloat16(f); return __bfloat162float(b); }
__device__ __forceinline__ u16 f2bf(float f) { __hip_bfloat16 b = __float2bfloat16(f); return *(u16*)&b; }

__global__ void k_mod(const u16* c, const u16* wmod, float* mod) {
    __shared__ float sc[2][D];
    for (int i = threadIdx.x; i < 2 * D; i += blockDim.x) { float v = bf2f(c[i]); sc[i / D][i % D] = rbf(v / (1.f + __expf(-v))); }
    __syncthreads();
    int n = blockIdx.x * blockDim.x + threadIdx.x;
    if (n >= 6 * D) return;
    float a0 = 0, a1 = 0;
    for (int k = 0; k < D; ++k) { float w = bf2f(wmod[(size_t)k * 6 * D + n]); a0 += sc[0][k] * w; a1 += sc[1][k] * w; }
    mod[n] = rbf(a0); mod[6 * D + n] = rbf(a1);
}

__device__ void ln_mod(const float* xs  , const float* mod, int si, int ci, float (*h)[D]) {
    __shared__ float st[2][2];
    if (threadIdx.x < 2) {
        int b = threadIdx.x; float s = 0; for (int i = 0; i < D; ++i) s += xs[b * D + i];
        float m = s / D; float v = 0; for (int i = 0; i < D; ++i) { float d = xs[b * D + i] - m; v += d * d; }
        v /= D; st[b][0] = rbf(m); st[b][1] = rbf(sqrtf(rbf(rbf(v) + 1e-5f)));
    }
    __syncthreads();
    for (int i = threadIdx.x; i < 2 * D; i += blockDim.x) {
        int b = i / D, j = i % D;
        float ln = rbf(rbf(xs[i] - st[b][0]) / st[b][1]);
        float t = rbf(ln * rbf(1.f + mod[b * 6 * D + ci * D + j]));
        h[b][j] = rbf(t + mod[b * 6 * D + si * D + j]);
    }
    __syncthreads();
}

__global__ void k_q(const u16* x, const float* mod, const u16* wq, float* q) {
    __shared__ float xs[2 * D]; __shared__ float h[2][D];
    for (int i = threadIdx.x; i < 2 * D; i += blockDim.x) xs[i] = bf2f(x[i]);
    __syncthreads();
    ln_mod(xs, mod, 0, 1, h);
    int n = blockIdx.x * blockDim.x + threadIdx.x;
    float a0 = 0, a1 = 0;
    for (int k = 0; k < D; ++k) { float w = bf2f(wq[(size_t)k * HQ * DH + n]); a0 += h[0][k] * w; a1 += h[1][k] * w; }
    q[n] = rbf(a0); q[HQ * DH + n] = rbf(a1);
}

__global__ void k_attn(const float* q, const u16* kc, const u16* vc, float* po, float* pml) {
    int s = blockIdx.x, kvh = blockIdx.y, b = blockIdx.z;
    __shared__ float qs[2][DH]; __shared__ float sc[2][KPS]; __shared__ float red[2][2];
    for (int i = threadIdx.x; i < 2 * DH; i += blockDim.x) qs[i / DH][i % DH] = q[b * HQ * DH + kvh * 2 * DH + i];
    __syncthreads();
    const u16* kb = kc + ((size_t)(b * HKV + kvh) * SK + (SK - W) + s * KPS) * DH;
    const u16* vb = vc + ((size_t)(b * HKV + kvh) * SK + (SK - W) + s * KPS) * DH;
    { int key = threadIdx.x % KPS, hh = threadIdx.x / KPS; float a = 0;
      for (int d = 0; d < DH; ++d) a += qs[hh][d] * bf2f(kb[key * DH + d]);
      sc[hh][key] = rbf(a) * 0.0625f; }
    __syncthreads();
    if (threadIdx.x < 2) { int hh = threadIdx.x; float m = -INFINITY; for (int i = 0; i < KPS; ++i) m = fmaxf(m, sc[hh][i]);
        float l = 0; for (int i = 0; i < KPS; ++i) { float p = __expf(sc[hh][i] - m); sc[hh][i] = p; l += p; } red[hh][0] = m; red[hh][1] = l; }
    __syncthreads();
    int d = threadIdx.x; float a0 = 0, a1 = 0;
    for (int k = 0; k < KPS; ++k) { float v = bf2f(vb[k * DH + d]); a0 += sc[0][k] * v; a1 += sc[1][k] * v; }
    int r0 = (b * HQ + kvh * 2) * NSPLIT + s, r1 = (b * HQ + kvh * 2 + 1) * NSPLIT + s;
    po[r0 * DH + d] = a0; po[r1 * DH + d] = a1;
    if (threadIdx.x < 2) { int r = threadIdx.x ? r1 : r0; pml[r * 2] = red[threadIdx.x][0]; pml[r * 2 + 1] = red[threadIdx.x][1]; }
}

__global__ void k_comb(const float* po, const float* pml, float* o) {
    int r = blockIdx.x, d = threadIdx.x; float M = -INFINITY;
    for (int s = 0; s < NSPLIT; ++s) M = fmaxf(M, pml[(r * NSPLIT + s) * 2]);
    float L = 0, a = 0;
    for (int s = 0; s < NSPLIT; ++s) { float w = __expf(pml[(r * NSPLIT + s) * 2] - M); L += w * pml[(r * NSPLIT + s) * 2 + 1]; a += w * po[(r * NSPLIT + s) * DH + d]; }
    o[r * DH + d] = rbf(a / L);
}

__global__ void k_wo(const float* o, const u16* wo, const u16* x, const float* mod, float* x1) {
    __shared__ float red[2][32][8];
    int cn = threadIdx.x % 8, ks = threadIdx.x / 8, n = blockIdx.x * 8 + cn;
    float a0 = 0, a1 = 0;
    for (int k = ks; k < HQ * DH; k += 32) { float w = bf2f(wo[(size_t)k * D + n]); a0 += o[k] * w; a1 += o[HQ * DH + k] * w; }
    red[0][ks][cn] = a0; red[1][ks][cn] = a1;
    __syncthreads();
    if (threadIdx.x < 16) { int b = threadIdx.x / 8; float s = 0; for (int i = 0; i < 32; ++i) s += red[b][i][cn];
        float att = rbf(s); x1[b * D + n] = rbf(bf2f(x[b * D + n]) + rbf(mod[b * 6 * D + 2 * D + n] * att)); }
}

__global__ void k_w1(const float* x1, const float* mod, const u16* w1, float* h1) {
    __shared__ float xs[2 * D]; __shared__ float h[2][D];
    for (int i = threadIdx.x; i < 2 * D; i += blockDim.x) xs[i] = x1[i];
    __syncthreads();
    ln_mod(xs, mod, 3, 4, h);
    int n = blockIdx.x * blockDim.x + threadIdx.x;
    float a0 = 0, a1 = 0;
    for (int k = 0; k < D; ++k) { float w = bf2f(w1[(size_t)k * F + n]); a0 += h[0][k] * w; a1 += h[1][k] * w; }
    a0 = rbf(a0); a1 = rbf(a1);
    h1[n] = rbf(a0 / (1.f + __expf(-a0))); h1[F + n] = rbf(a1 / (1.f + __expf(-a1)));
}

__global__ void k_w2(const float* h1, const u16* w2, const float* x1, const float* mod, u16* out) {
    __shared__ float red[2][32][8];
    int cn = threadIdx.x % 8, ks = threadIdx.x / 8, n = blockIdx.x * 8 + cn;
    float a0 = 0, a1 = 0;
    for (int k = ks; k < F; k += 32) { float w = bf2f(w2[(size_t)k * D + n]); a0 += h1[k] * w; a1 += h1[F + k] * w; }
    red[0][ks][cn] = a0; red[1][ks][cn] = a1;
    __syncthreads();
    if (threadIdx.x < 16) { int b = threadIdx.x / 8; float s = 0; for (int i = 0; i < 32; ++i) s += red[b][i][cn];
        float f = rbf(s); out[b * D + n] = f2bf(x1[b * D + n] + rbf(mod[b * 6 * D + 5 * D + n] * f)); }
}

extern "C" void kernel_launch(void* const* d_in, const int* in_sizes, int n_in, void* d_out, int out_size, void* d_ws, size_t ws_size, hipStream_t stream) {
    const u16* x = (const u16*)d_in[0]; const u16* kc = (const u16*)d_in[1]; const u16* vc = (const u16*)d_in[2];
    const u16* wq = (const u16*)d_in[3]; const u16* wo = (const u16*)d_in[4];
    const u16* w1 = (const u16*)d_in[6]; const u16* w2 = (const u16*)d_in[7]; const u16* c = (const u16*)d_in[8]; const u16* wmod = (const u16*)d_in[9];
    float* ws = (float*)d_ws;
    float* mod = ws; float* q = mod + 8192; float* po = q + 8192; float* pml = po + B * HQ * NSPLIT * DH; float* o = pml + 4096; float* x1 = o + 8192; float* h1 = x1 + 1024;
    k_mod<<<42, 64, 0, stream>>>(c, wmod, mod);
    k_q<<<64, 64, 0, stream>>>(x, mod, wq, q);
    k_attn<<<dim3(NSPLIT, HKV, B), 256, 0, stream>>>(q, kc, vc, po, pml);
    k_comb<<<B * HQ, 256, 0, stream>>>(po, pml, o);
    k_wo<<<56, 256, 0, stream>>>(o, wo, x, mod, x1);
    k_w1<<<28, 64, 0, stream>>>(x1, mod, w1, h1);
    k_w2<<<56, 256, 0, stream>>>(h1, w2, x1, mod, (u16*)d_out);
}
```

```cpp
#include <hip/hip_runtime.h>

typedef unsigned u32;
typedef unsigned long long u64;
typedef unsigned short u16;
typedef u32 v4u __attribute__((ext_vector_type(4)));
typedef u32 v2u __attribute__((ext_vector_type(2)));
typedef float v4f __attribute__((ext_vector_type(4)));
typedef float v2f __attribute__((ext_vector_type(2)));
typedef __bf16 v8bf __attribute__((ext_vector_type(8)));
typedef __bf16 v2bf __attribute__((ext_vector_type(2)));
typedef __amdgpu_buffer_rsrc_t rsrc_t;

#define WIN 5
#define ESZ 0x15171718u
#define LSZ 0x14131312u
#define QSLEEP 80

constexpr u32 WS_MOD = 4096;
constexpr u32 WS_Q = 32768;
constexpr u32 WS_ST = 65536;
constexpr u32 WS_X1 = 73728;
constexpr u32 WS_WP = 81920;
constexpr u32 WS_FP = 540672;
constexpr u32 WS_O = 1048576;
constexpr u32 WS_END = 2097152;
constexpr u32 OOB = 0x40000000u;
#define SPIN_CAP_V 400000u
constexpr u32 SPIN_CAP = SPIN_CAP_V;

constexpr int L_HV = 0;
constexpr int L_P1 = 2304;
constexpr int L_ST = L_P1 + 3584;
constexpr int L_QS = L_ST + 128;
constexpr int L_MX = L_QS + 1024;
constexpr int L_LS = L_MX + 64;
constexpr int L_PW = L_LS + 64;
constexpr int L_OP = L_PW + 2048;
constexpr int L_G2 = L_OP + 14336;
constexpr int L_END = L_G2 + 36864;

#define RS(p, n) __builtin_amdgcn_make_buffer_rsrc((void*)(p), (short)0, (int)(n), 0x00020000)
#define LD_NT(rs, vo, so) __builtin_amdgcn_raw_buffer_load_b128(rs, (int)(vo), (int)(so), 2)
#define LD_C4(rs, vo) __builtin_amdgcn_raw_buffer_load_b128(rs, (int)(vo), 0, 16)
#define LD_C2(rs, vo) __builtin_amdgcn_raw_buffer_load_b64(rs, (int)(vo), 0, 16)
#define ST_C4(v, rs, vo) __builtin_amdgcn_raw_buffer_store_b128(v, rs, (int)(vo), 0, 16)
#define ST_C2(v, rs, vo) __builtin_amdgcn_raw_buffer_store_b64(v, rs, (int)(vo), 0, 16)
#define FENCE() asm volatile("" ::: "memory")
#define LDSWAIT() asm volatile("s_waitcnt lgkmcnt(0)" ::: "memory")
#define BAR() do { asm volatile("s_waitcnt lgkmcnt(0)\n\ts_barrier" ::: "memory"); } while (0)

__device__ __forceinline__ float asf(u32 x) { return __builtin_bit_cast(float, x); }
__device__ __forceinline__ u32 asu(float x) { return __builtin_bit_cast(u32, x); }
__device__ __forceinline__ float lo16(u32 x) { return asf(x << 16); }
__device__ __forceinline__ float hi16(u32 x) { return asf(x & 0xffff0000u); }
__device__ __forceinline__ u32 pk(float a, float b) { v2bf v = {(__bf16)a, (__bf16)b}; return __builtin_bit_cast(u32, v); }
__device__ __forceinline__ float rb(float a) { return (float)(__bf16)a; }
template <int C> __device__ __forceinline__ float dpp(float x) { return asf((u32)__builtin_amdgcn_update_dpp(0, (int)asu(x), C, 0xf, 0xf, true)); }
template <int C> __device__ __forceinline__ u32 dppu(u32 x) { return (u32)__builtin_amdgcn_update_dpp(0, (int)x, C, 0xf, 0xf, true); }
constexpr int QX1 = 0xB1, QX2 = 0x4E, HMIR = 0x141, ROR4 = 0x124, ROR8 = 0x128;
__device__ __forceinline__ float x16_add(float x) { auto s = __builtin_amdgcn_permlane16_swap(asu(x), asu(x), false, false); return asf(s[0]) + asf(s[1]); }
__device__ __forceinline__ float x32_add(float x) { auto s = __builtin_amdgcn_permlane32_swap(asu(x), asu(x), false, false); return asf(s[0]) + asf(s[1]); }
__device__ __forceinline__ float x16_max(float x) { auto s = __builtin_amdgcn_permlane16_swap(asu(x), asu(x), false, false); return fmaxf(asf(s[0]), asf(s[1])); }
__device__ __forceinline__ float x32_max(float x) { auto s = __builtin_amdgcn_permlane32_swap(asu(x), asu(x), false, false); return fmaxf(asf(s[0]), asf(s[1])); }
__device__ __forceinline__ float sum_hi3(float x) { x += dpp<ROR8>(x); return x32_add(x16_add(x)); }
__device__ __forceinline__ float max_hi3(float x) { x = fmaxf(x, dpp<ROR8>(x)); return x32_max(x16_max(x)); }
__device__ __forceinline__ float sum_lo3(float x) { x += dpp<QX1>(x); x += dpp<QX2>(x); x += dpp<HMIR>(x); return x; }
__device__ __forceinline__ float max_lo3(float x) { x = fmaxf(x, dpp<QX1>(x)); x = fmaxf(x, dpp<QX2>(x)); x = fmaxf(x, dpp<HMIR>(x)); return x; }
__device__ __forceinline__ float sum_all(float x) { return sum_hi3(sum_lo3(x)); }
__device__ __forceinline__ v4f mfma(v4u a, v4u b, v4f c) {
    return __builtin_amdgcn_mfma_f32_16x16x32_bf16(__builtin_bit_cast(v8bf, a), __builtin_bit_cast(v8bf, b), c, 0, 0, 0);
}
__device__ __forceinline__ bool all_ok(bool ok) { return __builtin_amdgcn_ballot_w64(!ok) == 0ull; }
__device__ __forceinline__ float silu(float v) { return v * __builtin_amdgcn_rcpf(1.f + __builtin_amdgcn_exp2f(v * -1.44269504089f)); }

__device__ __forceinline__ void tr8(const v4u* r, v4u* o) {
#pragma unroll
    for (int j = 0; j < 8; ++j) {
        const u32 sel = (j & 1) ? 0x07060302u : 0x05040100u;
#pragma unroll
        for (int t = 0; t < 4; ++t) o[j][t] = __builtin_amdgcn_perm(r[2 * t + 1][j >> 1], r[2 * t][j >> 1], sel);
    }
}

#define STAMP(i)
#define DUMP()

__device__ __forceinline__ void ln_norm(char* lds, int w, int l, float& a0, float& a1) {
    float m0 = sum_all(a0) * (1.f / 64), m1 = sum_all(a1) * (1.f / 64);
    float d0 = a0 - m0, d1 = a1 - m1;
    float q0 = sum_all(d0 * d0), q1 = sum_all(d1 * d1);
    if (l == 0) *(v4f*)(lds + L_ST + w * 16) = v4f{m0, m1, q0, q1};
    BAR();
    v4f s = *(const v4f*)(lds + L_ST + (l & 7) * 16);
    const bool live = (l & 7) != 7;
    float M0 = sum_lo3(live ? s.x : 0.f) * (1.f / 7), M1 = sum_lo3(live ? s.y : 0.f) * (1.f / 7);
    float e0 = s.x - M0, e1 = s.y - M1;
    float V0 = sum_lo3(live ? s.z + 64.f * e0 * e0 : 0.f) * (1.f / 448), V1 = sum_lo3(live ? s.w + 64.f * e1 * e1 : 0.f) * (1.f / 448);
    float r0 = __builtin_amdgcn_rcpf(rb(__builtin_amdgcn_sqrtf(rb(rb(V0) + 1e-5f))));
    float r1 = __builtin_amdgcn_rcpf(rb(__builtin_amdgcn_sqrtf(rb(rb(V1) + 1e-5f))));
    a0 = rb(rb(a0 - rb(M0)) * r0);
    a1 = rb(rb(a1 - rb(M1)) * r1);
}

__device__ __forceinline__ void g1_block(char* lds, int w, int l, u32 hv, const v4u* B) {
    char* hb = lds + L_HV + w * 272;
    *(u16*)(hb + l * 2) = (u16)hv;
    *(u16*)(hb + 128 + l * 2) = (u16)(hv >> 16);
    const int row = l & 15, real = (row & 1) * 128 + ((l >> 4) * 2 + ((row >> 1) & 1)) * 16;
    LDSWAIT();
    v4f a0 = {0, 0, 0, 0}, a1 = {0, 0, 0, 0};
#pragma unroll
    for (int qd = 0; qd < 4; ++qd) {
        v4u A = *(const v4u*)(hb + ((row >> 2) == qd ? real : 256));
        a0 = mfma(A, B[2 * qd], a0);
        a1 = mfma(A, B[2 * qd + 1], a1);
    }
    v4f t = {a0[0] + dpp<ROR8>(a0[2]), a0[1] + dpp<ROR8>(a0[3]), a1[0] + dpp<ROR8>(a1[2]), a1[1] + dpp<ROR8>(a1[3])};
    if (row < 8) *(v4f*)(lds + L_P1 + w * 512 + (row * 8 + (l >> 4) * 2) * 8) = t;
}

__global__ void __launch_bounds__(512, 2)
adaln_block_kernel(const char* xin, const char* kc, const char* vc, const char* wq, const char* wo, const char* w1, const char* w2,
                   const char* cin, const char* wmod, char* out, char* ws, const u32* __restrict__ epoch) {
    __shared__ __attribute__((aligned(16))) char lds[L_END];
    const int l = threadIdx.x & 63;
    const int w = __builtin_amdgcn_readfirstlane(threadIdx.x >> 6);
    STAMP(0);
    const int xc = blockIdx.x & 7, idx = blockIdx.x >> 3, late = xc >> 2, pr = xc & 3;
    const int rel = idx - (late ? 0 : 4);
    const bool isX = rel < 0, isQ = rel >= 0 && rel < 8;
    const int ai = rel - 8;
    const int e = (ESZ >> (pr * 8)) & 255, lt = (LSZ >> (pr * 8)) & 255;
    const int t4 = rel & 3, jj = (rel >> 2) - 2;
    int g0, ng, split;
    {
        const int base = 5 * e + 6 * lt, R = 256 - base, k = late * 2 + (rel >> 2);
        if (isQ) { g0 = base + ((R * k) >> 2); ng = base + ((R * (k + 1)) >> 2) - g0; split = 11 + k; }
        else if (late) { g0 = 5 * e + jj * lt; ng = lt; split = 5 + jj; }
        else { g0 = jj * e; ng = e; split = jj; }
    }
    const int pair = pr * 4 + t4;
    const size_t kvoff = ((size_t)(((pair & 1) * 8 + (pair >> 1)) * 8192 + 6144) * 512) + (size_t)g0 * 4096;
    const int qn = xc * 8 + rel;
    int unit = idx * 4 + pr;
    if (isX && unit >= 14) return;
    bool front = isX || isQ;

    const rsrc_t rsW = RS(ws, WS_END);
    const int col = w * 64 + l;
    u32 xi0, xi1;
    {
        const rsrc_t rsv = RS(isX ? cin : xin, (front && w < 7) ? 1792 : 0);
        xi0 = __builtin_amdgcn_raw_buffer_load_b16(rsv, col * 2, 0, 0);
        xi1 = __builtin_amdgcn_raw_buffer_load_b16(rsv, col * 2 + 896, 0, 0);
    }
    FENCE();
    u32 pfv = 0;
    if (w == 7) {
        u64 pc; u32 len;
        asm volatile("s_getpc_b64 %0\n.Ladaln_pc:\n\ts_max_i32 %1, .Ladaln_e0-.Ladaln_pc, 0\n\ts_max_i32 %1, .Ladaln_e1-.Ladaln_pc, %1" : "=s"(pc), "=s"(len) :: "scc");
        const rsrc_t rsc = RS(pc, len);
        pfv = __builtin_amdgcn_raw_buffer_load_b32(rsc, l < 16 ? ((idx & 15) * 16 + l) * 128 : OOB, 0, 0);
    }
    u32 tag;
    v4u R[8];
    for (;;) {
        {
            const u32 pitch = isX ? 5376u : 8192u;
            const u32 c0 = (isX ? unit : qn) * 128u;
            const char* bp = front ? (isX ? wmod : wq) + c0 : kc + kvoff;
            const u32 nrec = front ? 448u * pitch - c0 : (u32)ng * 4096u;
            const rsrc_t rs0 = RS(bp, nrec);
            const u32 vo = front ? (l >> 3) * 8 * pitch + (l & 7) * 16 : (l & 7) * 512 + ((l >> 3) & 1) * 64 + (l >> 4) * 16;
            const u32 sb = w == 7 ? OOB : (front ? w * 64 * pitch : w * 4096u);
            const u32 s1 = front ? pitch : 128u, s2 = front ? 4 * pitch : 28672u;
#pragma unroll
            for (int i = 0; i < 8; ++i) R[i] = LD_NT(rs0, vo, sb + (i & 3) * s1 + (i >> 2) * s2);
        }
        FENCE();
        tag = *epoch * 1664525u + 1013904223u;
        asm volatile("" : "+s"(tag));
        if (!front) break;
        if (l < 4) *(u32*)(lds + L_HV + w * 272 + 256 + l * 4) = 0;
        float a0 = lo16(xi0), a1 = lo16(xi1);
        u32 hv;
        v4u B[8];
        if (isX) {
            hv = pk(silu(a0), silu(a1));
            tr8(R, B);
        } else {
            ln_norm(lds, w, l, a0, a1);
            tr8(R, B);
            STAMP(1);
            hv = 0;
            if (w < 7) {
                v2u sa, ca;
                for (u32 sp = 0; sp < SPIN_CAP; ++sp) {
                    sa = LD_C2(rsW, WS_MOD + col * 8);
                    ca = LD_C2(rsW, WS_MOD + 3584 + col * 8);
                    FENCE();
                    if (all_ok(sa.y == tag && ca.y == tag)) break;
                }
                hv = pk(rb(a0 * rb(1.f + lo16(ca.x))) + lo16(sa.x), rb(a1 * rb(1.f + hi16(ca.x))) + hi16(sa.x));
            }
        }
        STAMP(2);
        if (w < 7) g1_block(lds, w, l, hv, B);
        BAR();
        if (w == 7) {
            v2f s = *(const v2f*)(lds + L_P1 + l * 8);
#pragma unroll
            for (int i = 1; i < 7; ++i) s += *(const v2f*)(lds + L_P1 + i * 512 + l * 8);
            if (isX) {
                ST_C2((v2u{pk(s.x, s.y), tag}), rsW, WS_MOD + (unit * 64 + l) * 8);
            } else {
                const float give = (l & 1) ? s.x : s.y, keep = (l & 1) ? s.y : s.x;
                const float got = dpp<QX1>(give);
                const u32 v = (l & 1) ? pk(got, keep) : pk(keep, got);
                ST_C2((v2u{v, tag}), rsW, WS_Q + (l & 1) * 16384 + (qn * 32 + (l >> 1)) * 8);
            }
            if (unit < 14) STAMP(3); else STAMP(4);
        }
        BAR();
        if (isX) {
            unit += 14;
            if (unit >= 42) { asm volatile("" :: "v"(pfv)); DUMP(); return; }
        } else front = false;
    }

    const bool isF = !isQ && ai >= 7 && ai < 11 && (ai - 7) * 8 + xc < 28;
    const bool isR = !isQ && ai < 7;
    const int fu = (ai - 7) * 8 + xc;
    v4u G[8];
    const rsrc_t rsG = RS(isQ ? wo + (size_t)qn * 57344 : w2 + (size_t)(isF ? fu : 0) * 57344, (isQ || isF) ? 57344 : 0);
    const u32 gvo = l < 56 ? l * 16 : OOB;
    if (w < 7) {
        v4u K2[8], V[16];
        {
            const rsrc_t rsK = RS(kc + kvoff, ng * 4096), rsV = RS(vc + kvoff, ng * 4096);
            const u32 vo = (l & 7) * 512 + ((l >> 3) & 1) * 64 + (l >> 4) * 16;
#pragma unroll
            for (int i = 0; i < 8; ++i) {
                asm volatile("s_waitcnt vmcnt(%0)" :: "i"(WIN - 1) : "memory");
                K2[i] = LD_NT(rsK, vo + (i & 3) * 128, w * 4096 + (2 + (i >> 2)) * 28672);
            }
#pragma unroll
            for (int i = 0; i < 16; ++i) {
                asm volatile("s_waitcnt vmcnt(%0)" :: "i"(WIN - 1) : "memory");
                V[i] = LD_NT(rsV, l * 16 + (i & 3) * 1024, w * 4096 + (i >> 2) * 28672);
            }
            FENCE();
        }
        STAMP(5);
        char* pw = lds + L_PW + w * 256;
        if (l < 32) *(u32*)(pw + 128 + l * 4) = 0;
        v4u BV[8];
        tr8(V, BV);
        BAR();
        STAMP(6);
        const int row = l & 15, kq = l >> 4;
        v4f sc[4];
        {
            const char* qb = lds + L_QS + (row & 1) * 512 + ((row >> 1) & 1) * 64 + kq * 16;
#pragma unroll
            for (int g = 0; g < 4; ++g) sc[g] = v4f{0, 0, 0, 0};
#pragma unroll
            for (int j = 0; j < 4; ++j) {
                const v4u A = *(const v4u*)(qb + j * 128);
#pragma unroll
                for (int g = 0; g < 4; ++g) sc[g] = mfma(A, g < 2 ? R[g * 4 + j] : K2[(g - 2) * 4 + j], sc[g]);
            }
        }
        const v4f sg = kq == 0 ? sc[0] : kq == 1 ? sc[1] : kq == 2 ? sc[2] : sc[3];
        float s = (row < 8 ? sg[0] : sg[3]) + dpp<ROR8>(row < 8 ? sg[1] : sg[2]);
        s = (kq * 7 + w < ng) ? rb(s) : -INFINITY;
        {
            float m = x32_max(x16_max(max_lo3(s)));
            if ((l & 55) == 0) *(float*)(lds + L_MX + (l >> 3) * 32 + w * 4) = m;
        }
        BAR();
        float p;
        {
            const v4f ma = *(const v4f*)(lds + L_MX + ((l >> 3) & 1) * 32), mb = *(const v4f*)(lds + L_MX + ((l >> 3) & 1) * 32 + 16);
            const float M = fmaxf(fmaxf(fmaxf(ma[0], ma[1]), fmaxf(ma[2], ma[3])), fmaxf(fmaxf(mb[0], mb[1]), mb[2]));
            p = __builtin_amdgcn_exp2f((s - M) * (0.0625f * 1.44269504089f));
        }
        *(u16*)(pw + (kq >> 1) * 64 + (l & 1) * 32 + ((l >> 3) & 1) * 16 + ((kq & 1) * 4 + ((l >> 1) & 3)) * 2) = (u16)pk(p, p);
        {
            float ls = x32_add(x16_add(sum_lo3(p)));
            if ((l & 55) == 0) *(float*)(lds + L_LS + (l >> 3) * 32 + w * 4) = ls;
        }
        BAR();
        STAMP(7);
#pragma unroll
        for (int i = 0; i < 8; ++i) G[i] = LD_NT(rsG, gvo, (w * 8 + i) * 896);
        FENCE();
        v4f o0 = {0, 0, 0, 0}, o1 = {0, 0, 0, 0};
        {
            const int real = (kq >> 1) * 32 + (row & 1) * 16;
            const bool on = (kq & 1) == ((row >> 1) & 1);
            v4u A1[4];
#pragma unroll
            for (int qd = 0; qd < 4; ++qd) {
                const char* ap = pw + ((on && (row >> 2) == qd) ? real : 128);
                const v4u A = *(const v4u*)ap;
                A1[qd] = *(const v4u*)(ap + 64);
                o0 = mfma(A, BV[2 * qd], o0);
                o1 = mfma(A, BV[2 * qd + 1], o1);
            }
            tr8(V + 8, BV);
            STAMP(8);
#pragma unroll
            for (int qd = 0; qd < 4; ++qd) {
                o0 = mfma(A1[qd], BV[2 * qd], o0);
                o1 = mfma(A1[qd], BV[2 * qd + 1], o1);
            }
        }
        {
            char* op = lds + L_OP + w * 2048 + (row * 4 + kq) * 8;
#pragma unroll
            for (int r = 0; r < 4; ++r) *(v2f*)(op + r * 512) = v2f{o0[r], o1[r]};
        }
        BAR();
        if (w < 4) {
            v2f a = *(const v2f*)(lds + L_OP + w * 512 + l * 8);
#pragma unroll
            for (int i = 1; i < 7; ++i) a += *(const v2f*)(lds + L_OP + i * 2048 + w * 512 + l * 8);
            ST_C4((v4u{asu(a.x), tag, asu(a.y), tag}), rsW, WS_O + ((pair * 2 + (w & 1)) * 16 + split) * 2048 + (w >> 1) * 1024 + l * 16);
        }
        STAMP(9);
    } else {
        asm volatile("" :: "v"(pfv));
        if (!isQ) __builtin_amdgcn_s_sleep(QSLEEP);
        v4u qa, qb;
        const u32 qo = WS_Q + (pair & 1) * 16384 + (pair >> 1) * 2048 + l * 16;
        for (u32 sp = 0; sp < SPIN_CAP; ++sp) {
            qa = LD_C4(rsW, qo);
            qb = LD_C4(rsW, qo + 1024);
            FENCE();
            if (all_ok(qa.y == tag && qa.w == tag && qb.y == tag && qb.w == tag)) break;
        }
        *(v2u*)(lds + L_QS + l * 8) = v2u{qa.x, qa.z};
        *(v2u*)(lds + L_QS + 512 + l * 8) = v2u{qb.x, qb.z};
        STAMP(5);
        BAR();
        BAR();
        BAR();
        {
            const bool live = (l & 7) != 7;
            const float m = max_lo3(live ? *(const float*)(lds + L_MX + (l & 15) * 4) : -INFINITY);
            const float ls = sum_lo3(live ? *(const float*)(lds + L_LS + (l & 15) * 4) : 0.f);
            if ((l & 55) == 0) ST_C4((v4u{asu(m), tag, asu(ls), tag}), rsW, WS_ST + ((pair * 2 + (l >> 3)) * 16 + split) * 16);
        }
#pragma unroll
        for (int i = 0; i < 8; ++i) G[i] = LD_NT(rsG, gvo, (56 + i) * 896);
        FENCE();
        BAR();
    }

    if (isR) {
        if (w == 7) {
            const int r = xc * 7 + ai, c = r * 8 + (l & 7);
            const rsrc_t rsx = RS(xin, 1792), rso = RS(out, 1792);
            const float x0 = lo16(__builtin_amdgcn_raw_buffer_load_b16(rsx, c * 2, 0, 0)), x1 = lo16(__builtin_amdgcn_raw_buffer_load_b16(rsx, c * 2 + 896, 0, 0));
            const u32 touch = __builtin_amdgcn_raw_buffer_load_b16(rso, c * 2, 0, 0);
            v2u ga, gb;
            for (u32 sp = 0; sp < SPIN_CAP; ++sp) {
                ga = LD_C2(rsW, WS_MOD + (896 + c) * 8);
                gb = LD_C2(rsW, WS_MOD + (2240 + c) * 8);
                FENCE();
                if (all_ok(ga.y == tag && gb.y == tag)) break;
            }
            asm volatile("" :: "v"(touch));
            STAMP(10);
            const u32 po = (l >> 3) * 7168 + c * 16;
            float s0, s1;
            for (u32 sp = 0; sp < SPIN_CAP; ++sp) {
                v4u v[8];
#pragma unroll
                for (int i = 0; i < 8; ++i) v[i] = LD_C4(rsW, WS_WP + po + i * 57344);
                FENCE();
                bool ok = true; s0 = 0; s1 = 0;
#pragma unroll
                for (int i = 0; i < 8; ++i) { ok = ok && v[i].y == tag && v[i].w == tag; s0 += asf(v[i].x); s1 += asf(v[i].z); }
                if (all_ok(ok)) break;
            }
            STAMP(11);
            s0 = sum_hi3(s0); s1 = sum_hi3(s1);
            const u32 xn = pk(x0 + rb(lo16(ga.x) * rb(s0)), x1 + rb(hi16(ga.x) * rb(s1)));
            if (l < 8) ST_C2((v2u{xn, tag}), rsW, WS_X1 + c * 8);
            STAMP(12);
            for (u32 sp = 0; sp < SPIN_CAP; ++sp) {
                v4u v[4];
#pragma unroll
                for (int i = 0; i < 4; ++i) v[i] = LD_C4(rsW, (i == 3 && l >= 32) ? OOB : WS_FP + po + i * 57344);
                FENCE();
                bool ok = true; s0 = 0; s1 = 0;
#pragma unroll
                for (int i = 0; i < 4; ++i) { ok = ok && ((v[i].y == tag && v[i].w == tag) || (i == 3 && l >= 32)); s0 += asf(v[i].x); s1 += asf(v[i].z); }
                if (all_ok(ok)) break;
            }
            STAMP(13);
            s0 = sum_hi3(s0); s1 = sum_hi3(s1);
            const u32 on = pk(lo16(xn) + rb(lo16(gb.x) * rb(s0)), hi16(xn) + rb(hi16(gb.x) * rb(s1)));
            if (l < 16) __builtin_amdgcn_raw_buffer_store_b16((u16)(l < 8 ? on : on >> 16), rso, c * 2 + (l >> 3) * 896, 0, 0);
            if (r == 0 && l == 0) __builtin_amdgcn_raw_buffer_store_b32(tag, rsW, 0, 0, 16);
            STAMP(14);
            asm volatile(".Ladaln_e0:");
        }
        DUMP();
        return;
    }
    if (!isQ && !isF) { DUMP(); return; }

    u32 P;
    v4u BG[8];
    if (isQ) {
        tr8(G, BG);
        const int hq = qn >> 2, s0 = l >> 3;
        const u32 ph = (((hq >> 1) * 2 + (l & 1)) * 2 + (hq & 1)) * 16 + s0;
        const bool dead = s0 == 7;
        v4u sa, sb;
        for (u32 sp = 0; sp < SPIN_CAP; ++sp) {
            sa = LD_C4(rsW, WS_ST + ph * 16);
            sb = LD_C4(rsW, dead ? OOB : WS_ST + ph * 16 + 128);
            FENCE();
            if (all_ok(sa.y == tag && sa.w == tag && (dead || (sb.y == tag && sb.w == tag)))) break;
        }
        const float ma = asf(sa.x), mb = dead ? -INFINITY : asf(sb.x);
        const float M = max_hi3(fmaxf(ma, mb));
        float fa = __builtin_amdgcn_exp2f((ma - M) * (0.0625f * 1.44269504089f)), fb = __builtin_amdgcn_exp2f((mb - M) * (0.0625f * 1.44269504089f));
        const float inv = __builtin_amdgcn_rcpf(sum_hi3(fa * asf(sa.z) + fb * asf(sb.z)));
        fa *= inv; fb *= inv;
        STAMP(10);
        const u32 oo = WS_O + ph * 2048 + ((qn & 3) * 64 + w * 8 + ((l >> 1) & 3) * 2) * 8;
        v4u va, vb;
        for (u32 sp = 0; sp < SPIN_CAP; ++sp) {
            va = LD_C4(rsW, oo);
            vb = LD_C4(rsW, dead ? OOB : oo + 16384);
            FENCE();
            if (all_ok(va.y == tag && va.w == tag && (dead || (vb.y == tag && vb.w == tag)))) break;
        }
        STAMP(11);
        P = pk(sum_hi3(fa * asf(va.x) + fb * asf(vb.x)), sum_hi3(fa * asf(va.z) + fb * asf(vb.z)));
    } else {
        v4u B[8];
        {
            const rsrc_t rs1 = RS(w1 + fu * 128, 448 * 3584 - fu * 128);
            const u32 vo = (l >> 3) * 8 * 3584 + (l & 7) * 16;
#pragma unroll
            for (int i = 0; i < 8; ++i) R[i] = LD_NT(rs1, vo, (w * 64 + i) * 3584);
        }
        FENCE();
        if (l < 4) *(u32*)(lds + L_HV + w * 272 + 256 + l * 4) = 0;
        tr8(G, BG);
        tr8(R, B);
        v2u sb = {0, 0}, cb = {0, 0}, xg = {0, 0};
        if (w < 7) {
            for (u32 sp = 0; sp < SPIN_CAP; ++sp) {
                sb = LD_C2(rsW, WS_MOD + (1344 + col) * 8);
                cb = LD_C2(rsW, WS_MOD + (1792 + col) * 8);
                FENCE();
                if (all_ok(sb.y == tag && cb.y == tag)) break;
            }
            STAMP(10);
            for (u32 sp = 0; sp < SPIN_CAP; ++sp) {
                xg = LD_C2(rsW, WS_X1 + col * 8);
                FENCE();
                if (all_ok(xg.y == tag)) break;
            }
            STAMP(11);
        }
        float a0 = lo16(xg.x), a1 = hi16(xg.x);
        ln_norm(lds, w, l, a0, a1);
        if (w < 7) g1_block(lds, w, l, pk(rb(a0 * rb(1.f + lo16(cb.x))) + lo16(sb.x), rb(a1 * rb(1.f + hi16(cb.x))) + hi16(sb.x)), B);
        BAR();
        const bool dead = l >= 56;
        const float* pp = (const float*)(lds + L_P1 + (dead ? 0 : (l >> 3)) * 512 + (w * 8 + ((l >> 1) & 3) * 2) * 8 + (l & 1) * 4);
        const float h0 = rb(sum_hi3(dead ? 0.f : pp[0])), h1 = rb(sum_hi3(dead ? 0.f : pp[2]));
        P = pk(silu(h0), silu(h1));
    }
    STAMP(12);
    {
        const u32 Pr = dppu<ROR4>(P);
        const u32 lo = (l & 4) ? Pr : P, hi = (l & 4) ? P : Pr;
        const v4u A = {dppu<0x44>(lo), dppu<0xEE>(lo), dppu<0x44>(hi), dppu<0xEE>(hi)};
        const bool on = ((l >> 2) & 3) == (l >> 4);
        const u32 m0 = (on && !(l & 2)) ? ~0u : 0u, m1 = (on && (l & 2)) ? ~0u : 0u;
        const v4u A0 = A & m0, A1 = A & m1;
        char* gp = lds + L_G2 + w * 4608 + l * 16;
#pragma unroll
        for (int jl = 0; jl < 4; ++jl) {
            v4f a = mfma(A0, BG[2 * jl], v4f{0, 0, 0, 0});
            a = mfma(A1, BG[2 * jl + 1], a);
            *(v4f*)(gp + jl * 1152) = a;
        }
    }
    BAR();
    if (w < 7) {
        const char* gp = lds + L_G2 + ((l & 7) >> 1) * 1152 + (w * 8 + (l >> 3)) * 16 + (l & 1) * 8;
        v2f a = *(const v2f*)gp;
#pragma unroll
        for (int i = 1; i < 8; ++i) a += *(const v2f*)(gp + i * 4608);
        ST_C4((v4u{asu(a.x), tag, asu(a.y), tag}), rsW, (isQ ? WS_WP + qn * 7168 : WS_FP + fu * 7168) + col * 16);
    }
    asm volatile(".Ladaln_e1:");
    STAMP(13);
    DUMP();
}

extern "C" void kernel_launch(void* const* d_in, const int* in_sizes, int n_in, void* d_out, int out_size, void* d_ws, size_t ws_size,
                              hipStream_t stream) {
    adaln_block_kernel<<<256, 512, 0, stream>>>((const char*)d_in[0], (const char*)d_in[1], (const char*)d_in[2], (const char*)d_in[3],
                                                (const char*)d_in[4], (const char*)d_in[6], (const char*)d_in[7], (const char*)d_in[8],
                                                (const char*)d_in[9], (char*)d_out, (char*)d_ws, (const u32*)d_ws);
}
```

```cpp
#define PROBE 0
#include <hip/hip_runtime.h>

typedef unsigned u32;
typedef unsigned long long u64;
typedef unsigned short u16;
typedef u32 v4u __attribute__((ext_vector_type(4)));
typedef u32 v2u __attribute__((ext_vector_type(2)));
typedef float v4f __attribute__((ext_vector_type(4)));
typedef float v2f __attribute__((ext_vector_type(2)));
typedef __bf16 v8bf __attribute__((ext_vector_type(8)));
typedef __bf16 v2bf __attribute__((ext_vector_type(2)));
typedef __amdgpu_buffer_rsrc_t rsrc_t;

#define WIN 5
#define ESZ 0x15171718u
#define LSZ 0x14131312u
#define QSLEEP 80

constexpr u32 WS_MOD = 4096;
constexpr u32 WS_Q = 32768;
constexpr u32 WS_ST = 65536;
constexpr u32 WS_X1 = 73728;
constexpr u32 WS_WP = 81920;
constexpr u32 WS_FP = 540672;
constexpr u32 WS_O = 1048576;
constexpr u32 WS_END = 2097152;
constexpr u32 OOB = 0x40000000u;
#define SPIN_CAP_V 400000u
constexpr u32 SPIN_CAP = SPIN_CAP_V;

constexpr int L_HV = 0;
constexpr int L_P1 = 2304;
constexpr int L_ST = L_P1 + 3584;
constexpr int L_QS = L_ST + 128;
constexpr int L_MX = L_QS + 1024;
constexpr int L_LS = L_MX + 64;
constexpr int L_PW = L_LS + 64;
constexpr int L_OP = L_PW + 2048;
constexpr int L_G2 = L_OP + 14336;
constexpr int L_END = L_G2 + 36864;

#define RS(p, n) __builtin_amdgcn_make_buffer_rsrc((void*)(p), (short)0, (int)(n), 0x00020000)
#define LD_NT(rs, vo, so) __builtin_amdgcn_raw_buffer_load_b128(rs, (int)(vo), (int)(so), 2)
#define LD_C4(rs, vo) __builtin_amdgcn_raw_buffer_load_b128(rs, (int)(vo), 0, 16)
#define LD_C2(rs, vo) __builtin_amdgcn_raw_buffer_load_b64(rs, (int)(vo), 0, 16)
#define ST_C4(v, rs, vo) __builtin_amdgcn_raw_buffer_store_b128(v, rs, (int)(vo), 0, 16)
#define ST_C2(v, rs, vo) __builtin_amdgcn_raw_buffer_store_b64(v, rs, (int)(vo), 0, 16)
#define FENCE() asm volatile("" ::: "memory")
#define LDSWAIT() asm volatile("s_waitcnt lgkmcnt(0)" ::: "memory")
#define BAR() do { asm volatile("s_waitcnt lgkmcnt(0)\n\ts_barrier" ::: "memory"); } while (0)

__device__ __forceinline__ float asf(u32 x) { return __builtin_bit_cast(float, x); }
__device__ __forceinline__ u32 asu(float x) { return __builtin_bit_cast(u32, x); }
__device__ __forceinline__ float lo16(u32 x) { return asf(x << 16); }
__device__ __forceinline__ float hi16(u32 x) { return asf(x & 0xffff0000u); }
__device__ __forceinline__ u32 pk(float a, float b) { v2bf v = {(__bf16)a, (__bf16)b}; return __builtin_bit_cast(u32, v); }
__device__ __forceinline__ float rb(float a) { return (float)(__bf16)a; }
template <int C> __device__ __forceinline__ float dpp(float x) { return asf((u32)__builtin_amdgcn_update_dpp(0, (int)asu(x), C, 0xf, 0xf, true)); }
template <int C> __device__ __forceinline__ u32 dppu(u32 x) { return (u32)__builtin_amdgcn_update_dpp(0, (int)x, C, 0xf, 0xf, true); }
constexpr int QX1 = 0xB1, QX2 = 0x4E, HMIR = 0x141, ROR4 = 0x124, ROR8 = 0x128;
__device__ __forceinline__ float x16_add(float x) { auto s = __builtin_amdgcn_permlane16_swap(asu(x), asu(x), false, false); return asf(s[0]) + asf(s[1]); }
__device__ __forceinline__ float x32_add(float x) { auto s = __builtin_amdgcn_permlane32_swap(asu(x), asu(x), false, false); return asf(s[0]) + asf(s[1]); }
__device__ __forceinline__ float x16_max(float x) { auto s = __builtin_amdgcn_permlane16_swap(asu(x), asu(x), false, false); return fmaxf(asf(s[0]), asf(s[1])); }
__device__ __forceinline__ float x32_max(float x) { auto s = __builtin_amdgcn_permlane32_swap(asu(x), asu(x), false, false); return fmaxf(asf(s[0]), asf(s[1])); }
__device__ __forceinline__ float sum_hi3(float x) { x += dpp<ROR8>(x); return x32_add(x16_add(x)); }
__device__ __forceinline__ float max_hi3(float x) { x = fmaxf(x, dpp<ROR8>(x)); return x32_max(x16_max(x)); }
__device__ __forceinline__ float sum_lo3(float x) { x += dpp<QX1>(x); x += dpp<QX2>(x); x += dpp<HMIR>(x); return x; }
__device__ __forceinline__ float max_lo3(float x) { x = fmaxf(x, dpp<QX1>(x)); x = fmaxf(x, dpp<QX2>(x)); x = fmaxf(x, dpp<HMIR>(x)); return x; }
__device__ __forceinline__ float sum_all(float x) { return sum_hi3(sum_lo3(x)); }
__device__ __forceinline__ v4f mfma(v4u a, v4u b, v4f c) {
    return __builtin_amdgcn_mfma_f32_16x16x32_bf16(__builtin_bit_cast(v8bf, a), __builtin_bit_cast(v8bf, b), c, 0, 0, 0);
}
__device__ __forceinline__ bool all_ok(bool ok) { return __builtin_amdgcn_ballot_w64(!ok) == 0ull; }
__device__ __forceinline__ float silu(float v) { return v * __builtin_amdgcn_rcpf(1.f + __builtin_amdgcn_exp2f(v * -1.44269504089f)); }

__device__ __forceinline__ void tr8(const v4u* r, v4u* o) {
#pragma unroll
    for (int j = 0; j < 8; ++j) {
        const u32 sel = (j & 1) ? 0x07060302u : 0x05040100u;
#pragma unroll
        for (int t = 0; t < 4; ++t) o[j][t] = __builtin_amdgcn_perm(r[2 * t + 1][j >> 1], r[2 * t][j >> 1], sel);
    }
}

#define STAMP(i) do { if (i == 0 || i == PROBE) { ts[i ? 1 : 0] = __builtin_amdgcn_s_memrealtime(); if (i == 0) ts[2] = __builtin_amdgcn_s_memtime(); } } while (0)
#define DUMP()

__device__ __forceinline__ void ln_norm(char* lds, int w, int l, float& a0, float& a1) {
    float m0 = sum_all(a0) * (1.f / 64), m1 = sum_all(a1) * (1.f / 64);
    float d0 = a0 - m0, d1 = a1 - m1;
    float q0 = sum_all(d0 * d0), q1 = sum_all(d1 * d1);
    if (l == 0) *(v4f*)(lds + L_ST + w * 16) = v4f{m0, m1, q0, q1};
    BAR();
    v4f s = *(const v4f*)(lds + L_ST + (l & 7) * 16);
    const bool live = (l & 7) != 7;
    float M0 = sum_lo3(live ? s.x : 0.f) * (1.f / 7), M1 = sum_lo3(live ? s.y : 0.f) * (1.f / 7);
    float e0 = s.x - M0, e1 = s.y - M1;
    float V0 = sum_lo3(live ? s.z + 64.f * e0 * e0 : 0.f) * (1.f / 448), V1 = sum_lo3(live ? s.w + 64.f * e1 * e1 : 0.f) * (1.f / 448);
    float r0 = __builtin_amdgcn_rcpf(rb(__builtin_amdgcn_sqrtf(rb(rb(V0) + 1e-5f))));
    float r1 = __builtin_amdgcn_rcpf(rb(__builtin_amdgcn_sqrtf(rb(rb(V1) + 1e-5f))));
    a0 = rb(rb(a0 - rb(M0)) * r0);
    a1 = rb(rb(a1 - rb(M1)) * r1);
}

__device__ __forceinline__ void g1_block(char* lds, int w, int l, u32 hv, const v4u* B) {
    char* hb = lds + L_HV + w * 272;
    *(u16*)(hb + l * 2) = (u16)hv;
    *(u16*)(hb + 128 + l * 2) = (u16)(hv >> 16);
    const int row = l & 15, real = (row & 1) * 128 + ((l >> 4) * 2 + ((row >> 1) & 1)) * 16;
    LDSWAIT();
    v4f a0 = {0, 0, 0, 0}, a1 = {0, 0, 0, 0};
#pragma unroll
    for (int qd = 0; qd < 4; ++qd) {
        v4u A = *(const v4u*)(hb + ((row >> 2) == qd ? real : 256));
        a0 = mfma(A, B[2 * qd], a0);
        a1 = mfma(A, B[2 * qd + 1], a1);
    }
    v4f t = {a0[0] + dpp<ROR8>(a0[2]), a0[1] + dpp<ROR8>(a0[3]), a1[0] + dpp<ROR8>(a1[2]), a1[1] + dpp<ROR8>(a1[3])};
    if (row < 8) *(v4f*)(lds + L_P1 + w * 512 + (row * 8 + (l >> 4) * 2) * 8) = t;
}

__global__ void __launch_bounds__(512, 2)
adaln_block_kernel(const char* xin, const char* kc, const char* vc, const char* wq, const char* wo, const char* w1, const char* w2,
                   const char* cin, const char* wmod, char* out, char* ws, const u32* __restrict__ epoch) {
    __shared__ __attribute__((aligned(16))) char lds[L_END];
    const int l = threadIdx.x & 63;
    const int w = __builtin_amdgcn_readfirstlane(threadIdx.x >> 6);
    u64 ts[16];
#pragma unroll
    for (int i = 0; i < 16; ++i) ts[i] = 0;
    STAMP(0);
    const int xc = blockIdx.x & 7, idx = blockIdx.x >> 3, late = xc >> 2, pr = xc & 3;
    const int rel = idx - (late ? 0 : 4);
    const bool isX = rel < 0, isQ = rel >= 0 && rel < 8;
    const int ai = rel - 8;
    const int e = (ESZ >> (pr * 8)) & 255, lt = (LSZ >> (pr * 8)) & 255;
    const int t4 = rel & 3, jj = (rel >> 2) - 2;
    int g0, ng, split;
    {
        const int base = 5 * e + 6 * lt, R = 256 - base, k = late * 2 + (rel >> 2);
        if (isQ) { g0 = base + ((R * k) >> 2); ng = base + ((R * (k + 1)) >> 2) - g0; split = 11 + k; }
        else if (late) { g0 = 5 * e + jj * lt; ng = lt; split = 5 + jj; }
        else { g0 = jj * e; ng = e; split = jj; }
    }
    const int pair = pr * 4 + t4;
    const size_t kvoff = ((size_t)(((pair & 1) * 8 + (pair >> 1)) * 8192 + 6144) * 512) + (size_t)g0 * 4096;
    const int qn = xc * 8 + rel;
    int unit = idx * 4 + pr;
    if (isX && unit >= 14) return;
    bool front = isX || isQ;

    const rsrc_t rsW = RS(ws, WS_END);
    const int col = w * 64 + l;
    u32 xi0, xi1;
    {
        const rsrc_t rsv = RS(isX ? cin : xin, (front && w < 7) ? 1792 : 0);
        xi0 = __builtin_amdgcn_raw_buffer_load_b16(rsv, col * 2, 0, 0);
        xi1 = __builtin_amdgcn_raw_buffer_load_b16(rsv, col * 2 + 896, 0, 0);
    }
    FENCE();
    u32 pfv = 0;
    if (w == 7) {
        u64 pc; u32 len;
        asm volatile("s_getpc_b64 %0\n.Ladaln_pc:\n\ts_max_i32 %1, .Ladaln_e0-.Ladaln_pc, 0\n\ts_max_i32 %1, .Ladaln_e1-.Ladaln_pc, %1" : "=s"(pc), "=s"(len) :: "scc");
        const rsrc_t rsc = RS(pc, len);
        pfv = __builtin_amdgcn_raw_buffer_load_b32(rsc, l < 16 ? ((idx & 15) * 16 + l) * 128 : OOB, 0, 0);
    }
    u32 tag;
    v4u R[8];
    for (;;) {
        {
            const u32 pitch = isX ? 5376u : 8192u;
            const u32 c0 = (isX ? unit : qn) * 128u;
            const char* bp = front ? (isX ? wmod : wq) + c0 : kc + kvoff;
            const u32 nrec = front ? 448u * pitch - c0 : (u32)ng * 4096u;
            const rsrc_t rs0 = RS(bp, nrec);
            const u32 vo = front ? (l >> 3) * 8 * pitch + (l & 7) * 16 : (l & 7) * 512 + ((l >> 3) & 1) * 64 + (l >> 4) * 16;
            const u32 sb = w == 7 ? OOB : (front ? w * 64 * pitch : w * 4096u);
            const u32 s1 = front ? pitch : 128u, s2 = front ? 4 * pitch : 28672u;
#pragma unroll
            for (int i = 0; i < 8; ++i) R[i] = LD_NT(rs0, vo, sb + (i & 3) * s1 + (i >> 2) * s2);
        }
        FENCE();
        tag = *epoch * 1664525u + 1013904223u;
        asm volatile("" : "+s"(tag));
        if (!front) break;
        if (l < 4) *(u32*)(lds + L_HV + w * 272 + 256 + l * 4) = 0;
        float a0 = lo16(xi0), a1 = lo16(xi1);
        u32 hv;
        v4u B[8];
        if (isX) {
            hv = pk(silu(a0), silu(a1));
            tr8(R, B);
        } else {
            ln_norm(lds, w, l, a0, a1);
            tr8(R, B);
            STAMP(1);
            hv = 0;
            if (w < 7) {
                v2u sa, ca;
                for (u32 sp = 0; sp < SPIN_CAP; ++sp) {
                    sa = LD_C2(rsW, WS_MOD + col * 8);
                    ca = LD_C2(rsW, WS_MOD + 3584 + col * 8);
                    FENCE();
                    if (all_ok(sa.y == tag && ca.y == tag)) break;
                }
                hv = pk(rb(a0 * rb(1.f + lo16(ca.x))) + lo16(sa.x), rb(a1 * rb(1.f + hi16(ca.x))) + hi16(sa.x));
            }
        }
        STAMP(2);
        if (w < 7) g1_block(lds, w, l, hv, B);
        BAR();
        if (w == 7) {
            v2f s = *(const v2f*)(lds + L_P1 + l * 8);
#pragma unroll
            for (int i = 1; i < 7; ++i) s += *(const v2f*)(lds + L_P1 + i * 512 + l * 8);
            if (isX) {
                ST_C2((v2u{pk(s.x, s.y), tag}), rsW, WS_MOD + (unit * 64 + l) * 8);
            } else {
                const float give = (l & 1) ? s.x : s.y, keep = (l & 1) ? s.y : s.x;
                const float got = dpp<QX1>(give);
                const u32 v = (l & 1) ? pk(got, keep) : pk(keep, got);
                ST_C2((v2u{v, tag}), rsW, WS_Q + (l & 1) * 16384 + (qn * 32 + (l >> 1)) * 8);
            }
            if (unit < 14) STAMP(3); else STAMP(4);
        }
        BAR();
        if (isX) {
            unit += 14;
            if (unit >= 42) { asm volatile("" :: "v"(pfv)); DUMP(); return; }
        } else front = false;
    }

    const bool isF = !isQ && ai >= 7 && ai < 11 && (ai - 7) * 8 + xc < 28;
    const bool isR = !isQ && ai < 7;
    const int fu = (ai - 7) * 8 + xc;
    v4u G[8];
    const rsrc_t rsG = RS(isQ ? wo + (size_t)qn * 57344 : w2 + (size_t)(isF ? fu : 0) * 57344, (isQ || isF) ? 57344 : 0);
    const u32 gvo = l < 56 ? l * 16 : OOB;
    if (w < 7) {
        v4u K2[8], V[16];
        {
            const rsrc_t rsK = RS(kc + kvoff, ng * 4096), rsV = RS(vc + kvoff, ng * 4096);
            const u32 vo = (l & 7) * 512 + ((l >> 3) & 1) * 64 + (l >> 4) * 16;
#pragma unroll
            for (int i = 0; i < 8; ++i) {
                asm volatile("s_waitcnt vmcnt(%0)" :: "i"(WIN - 1) : "memory");
                K2[i] = LD_NT(rsK, vo + (i & 3) * 128, w * 4096 + (2 + (i >> 2)) * 28672);
            }
#pragma unroll
            for (int i = 0; i < 16; ++i) {
                asm volatile("s_waitcnt vmcnt(%0)" :: "i"(WIN - 1) : "memory");
                V[i] = LD_NT(rsV, l * 16 + (i & 3) * 1024, w * 4096 + (i >> 2) * 28672);
            }
            FENCE();
        }
        STAMP(5);
        char* pw = lds + L_PW + w * 256;
        if (l < 32) *(u32*)(pw + 128 + l * 4) = 0;
        v4u BV[8];
        tr8(V, BV);
        BAR();
        STAMP(6);
        const int row = l & 15, kq = l >> 4;
        v4f sc[4];
        {
            const char* qb = lds + L_QS + (row & 1) * 512 + ((row >> 1) & 1) * 64 + kq * 16;
#pragma unroll
            for (int g = 0; g < 4; ++g) sc[g] = v4f{0, 0, 0, 0};
#pragma unroll
            for (int j = 0; j < 4; ++j) {
                const v4u A = *(const v4u*)(qb + j * 128);
#pragma unroll
                for (int g = 0; g < 4; ++g) sc[g] = mfma(A, g < 2 ? R[g * 4 + j] : K2[(g - 2) * 4 + j], sc[g]);
            }
        }
        const v4f sg = kq == 0 ? sc[0] : kq == 1 ? sc[1] : kq == 2 ? sc[2] : sc[3];
        float s = (row < 8 ? sg[0] : sg[3]) + dpp<ROR8>(row < 8 ? sg[1] : sg[2]);
        s = (kq * 7 + w < ng) ? rb(s) : -INFINITY;
        {
            float m = x32_max(x16_max(max_lo3(s)));
            if ((l & 55) == 0) *(float*)(lds + L_MX + (l >> 3) * 32 + w * 4) = m;
        }
        BAR();
        float p;
        {
            const v4f ma = *(const v4f*)(lds + L_MX + ((l >> 3) & 1) * 32), mb = *(const v4f*)(lds + L_MX + ((l >> 3) & 1) * 32 + 16);
            const float M = fmaxf(fmaxf(fmaxf(ma[0], ma[1]), fmaxf(ma[2], ma[3])), fmaxf(fmaxf(mb[0], mb[1]), mb[2]));
            p = __builtin_amdgcn_exp2f((s - M) * (0.0625f * 1.44269504089f));
        }
        *(u16*)(pw + (kq >> 1) * 64 + (l & 1) * 32 + ((l >> 3) & 1) * 16 + ((kq & 1) * 4 + ((l >> 1) & 3)) * 2) = (u16)pk(p, p);
        {
            float ls = x32_add(x16_add(sum_lo3(p)));
            if ((l & 55) == 0) *(float*)(lds + L_LS + (l >> 3) * 32 + w * 4) = ls;
        }
        BAR();
        STAMP(7);
#pragma unroll
        for (int i = 0; i < 8; ++i) G[i] = LD_NT(rsG, gvo, (w * 8 + i) * 896);
        FENCE();
        v4f o0 = {0, 0, 0, 0}, o1 = {0, 0, 0, 0};
        {
            const int real = (kq >> 1) * 32 + (row & 1) * 16;
            const bool on = (kq & 1) == ((row >> 1) & 1);
            v4u A1[4];
#pragma unroll
            for (int qd = 0; qd < 4; ++qd) {
                const char* ap = pw + ((on && (row >> 2) == qd) ? real : 128);
                const v4u A = *(const v4u*)ap;
                A1[qd] = *(const v4u*)(ap + 64);
                o0 = mfma(A, BV[2 * qd], o0);
                o1 = mfma(A, BV[2 * qd + 1], o1);
            }
            tr8(V + 8, BV);
            STAMP(8);
#pragma unroll
            for (int qd = 0; qd < 4; ++qd) {
                o0 = mfma(A1[qd], BV[2 * qd], o0);
                o1 = mfma(A1[qd], BV[2 * qd + 1], o1);
            }
        }
        {
            char* op = lds + L_OP + w * 2048 + (row * 4 + kq) * 8;
#pragma unroll
            for (int r = 0; r < 4; ++r) *(v2f*)(op + r * 512) = v2f{o0[r], o1[r]};
        }
        BAR();
        if (w < 4) {
            v2f a = *(const v2f*)(lds + L_OP + w * 512 + l * 8);
#pragma unroll
            for (int i = 1; i < 7; ++i) a += *(const v2f*)(lds + L_OP + i * 2048 + w * 512 + l * 8);
            ST_C4((v4u{asu(a.x), tag, asu(a.y), tag}), rsW, WS_O + ((pair * 2 + (w & 1)) * 16 + split) * 2048 + (w >> 1) * 1024 + l * 16);
        }
        STAMP(9);
    } else {
        asm volatile("" :: "v"(pfv));
        if (!isQ) __builtin_amdgcn_s_sleep(QSLEEP);
        v4u qa, qb;
        const u32 qo = WS_Q + (pair & 1) * 16384 + (pair >> 1) * 2048 + l * 16;
        for (u32 sp = 0; sp < SPIN_CAP; ++sp) {
            qa = LD_C4(rsW, qo);
            qb = LD_C4(rsW, qo + 1024);
            FENCE();
            if (all_ok(qa.y == tag && qa.w == tag && qb.y == tag && qb.w == tag)) break;
        }
        *(v2u*)(lds + L_QS + l * 8) = v2u{qa.x, qa.z};
        *(v2u*)(lds + L_QS + 512 + l * 8) = v2u{qb.x, qb.z};
        STAMP(5);
        BAR();
        BAR();
        BAR();
        {
            const bool live = (l & 7) != 7;
            const float m = max_lo3(live ? *(const float*)(lds + L_MX + (l & 15) * 4) : -INFINITY);
            const float ls = sum_lo3(live ? *(const float*)(lds + L_LS + (l & 15) * 4) : 0.f);
            if ((l & 55) == 0) ST_C4((v4u{asu(m), tag, asu(ls), tag}), rsW, WS_ST + ((pair * 2 + (l >> 3)) * 16 + split) * 16);
        }
#pragma unroll
        for (int i = 0; i < 8; ++i) G[i] = LD_NT(rsG, gvo, (56 + i) * 896);
        FENCE();
        BAR();
        STAMP(9);
    }

    if (isR) {
        if (w == 7) {
            const int r = xc * 7 + ai, c = r * 8 + (l & 7);
            const rsrc_t rsx = RS(xin, 1792), rso = RS(out, 1792);
            const float x0 = lo16(__builtin_amdgcn_raw_buffer_load_b16(rsx, c * 2, 0, 0)), x1 = lo16(__builtin_amdgcn_raw_buffer_load_b16(rsx, c * 2 + 896, 0, 0));
            const u32 touch = __builtin_amdgcn_raw_buffer_load_b16(rso, c * 2, 0, 0);
            v2u ga, gb;
            for (u32 sp = 0; sp < SPIN_CAP; ++sp) {
                ga = LD_C2(rsW, WS_MOD + (896 + c) * 8);
                gb = LD_C2(rsW, WS_MOD + (2240 + c) * 8);
                FENCE();
                if (all_ok(ga.y == tag && gb.y == tag)) break;
            }
            asm volatile("" :: "v"(touch));
            STAMP(10);
            const u32 po = (l >> 3) * 7168 + c * 16;
            float s0, s1;
            for (u32 sp = 0; sp < SPIN_CAP; ++sp) {
                v4u v[8];
#pragma unroll
                for (int i = 0; i < 8; ++i) v[i] = LD_C4(rsW, WS_WP + po + i * 57344);
                FENCE();
                bool ok = true; s0 = 0; s1 = 0;
#pragma unroll
                for (int i = 0; i < 8; ++i) { ok = ok && v[i].y == tag && v[i].w == tag; s0 += asf(v[i].x); s1 += asf(v[i].z); }
                if (all_ok(ok)) break;
            }
            STAMP(11);
            s0 = sum_hi3(s0); s1 = sum_hi3(s1);
            const u32 xn = pk(x0 + rb(lo16(ga.x) * rb(s0)), x1 + rb(hi16(ga.x) * rb(s1)));
            if (l < 8) ST_C2((v2u{xn, tag}), rsW, WS_X1 + c * 8);
            STAMP(12);
            for (u32 sp = 0; sp < SPIN_CAP; ++sp) {
                v4u v[4];
#pragma unroll
                for (int i = 0; i < 4; ++i) v[i] = LD_C4(rsW, (i == 3 && l >= 32) ? OOB : WS_FP + po + i * 57344);
                FENCE();
                bool ok = true; s0 = 0; s1 = 0;
#pragma unroll
                for (int i = 0; i < 4; ++i) { ok = ok && ((v[i].y == tag && v[i].w == tag) || (i == 3 && l >= 32)); s0 += asf(v[i].x); s1 += asf(v[i].z); }
                if (all_ok(ok)) break;
            }
            STAMP(13);
            s0 = sum_hi3(s0); s1 = sum_hi3(s1);
            const u32 on = pk(lo16(xn) + rb(lo16(gb.x) * rb(s0)), hi16(xn) + rb(hi16(gb.x) * rb(s1)));
            if (l < 16) __builtin_amdgcn_raw_buffer_store_b16((u16)(l < 8 ? on : on >> 16), rso, c * 2 + (l >> 3) * 896, 0, 0);
            if (r == 0 && l == 0) __builtin_amdgcn_raw_buffer_store_b32(tag, rsW, 0, 0, 16);
            STAMP(14);
            if (r == 0) {
                u64 tgt = ts[0] + 6000 + (PROBE == 0 ? 0 : PROBE == 15 ? (__builtin_amdgcn_s_memtime() - ts[2]) / 10 : 4 * (ts[1] - ts[0]));
                while (__builtin_amdgcn_s_memrealtime() < tgt) __builtin_amdgcn_s_sleep(1);
            }
            asm volatile(".Ladaln_e0:");
        }
        DUMP();
        return;
    }
    if (!isQ && !isF) { DUMP(); return; }

    u32 P;
    v4u BG[8];
    if (isQ) {
        tr8(G, BG);
        const int hq = qn >> 2, s0 = l >> 3;
        const u32 ph = (((hq >> 1) * 2 + (l & 1)) * 2 + (hq & 1)) * 16 + s0;
        const bool dead = s0 == 7;
        v4u sa, sb;
        for (u32 sp = 0; sp < SPIN_CAP; ++sp) {
            sa = LD_C4(rsW, WS_ST + ph * 16);
            sb = LD_C4(rsW, dead ? OOB : WS_ST + ph * 16 + 128);
            FENCE();
            if (all_ok(sa.y == tag && sa.w == tag && (dead || (sb.y == tag && sb.w == tag)))) break;
        }
        const float ma = asf(sa.x), mb = dead ? -INFINITY : asf(sb.x);
        const float M = max_hi3(fmaxf(ma, mb));
        float fa = __builtin_amdgcn_exp2f((ma - M) * (0.0625f * 1.44269504089f)), fb = __builtin_amdgcn_exp2f((mb - M) * (0.0625f * 1.44269504089f));
        const float inv = __builtin_amdgcn_rcpf(sum_hi3(fa * asf(sa.z) + fb * asf(sb.z)));
        fa *= inv; fb *= inv;
        STAMP(10);
        const u32 oo = WS_O + ph * 2048 + ((qn & 3) * 64 + w * 8 + ((l >> 1) & 3) * 2) * 8;
        v4u va, vb;
        for (u32 sp = 0; sp < SPIN_CAP; ++sp) {
            va = LD_C4(rsW, oo);
            vb = LD_C4(rsW, dead ? OOB : oo + 16384);
            FENCE();
            if (all_ok(va.y == tag && va.w == tag && (dead || (vb.y == tag && vb.w == tag)))) break;
        }
        STAMP(11);
        P = pk(sum_hi3(fa * asf(va.x) + fb * asf(vb.x)), sum_hi3(fa * asf(va.z) + fb * asf(vb.z)));
    } else {
        v4u B[8];
        {
            const rsrc_t rs1 = RS(w1 + fu * 128, 448 * 3584 - fu * 128);
            const u32 vo = (l >> 3) * 8 * 3584 + (l & 7) * 16;
#pragma unroll
            for (int i = 0; i < 8; ++i) R[i] = LD_NT(rs1, vo, (w * 64 + i) * 3584);
        }
        FENCE();
        if (l < 4) *(u32*)(lds + L_HV + w * 272 + 256 + l * 4) = 0;
        tr8(G, BG);
        tr8(R, B);
        v2u sb = {0, 0}, cb = {0, 0}, xg = {0, 0};
        if (w < 7) {
            for (u32 sp = 0; sp < SPIN_CAP; ++sp) {
                sb = LD_C2(rsW, WS_MOD + (1344 + col) * 8);
                cb = LD_C2(rsW, WS_MOD + (1792 + col) * 8);
                FENCE();
                if (all_ok(sb.y == tag && cb.y == tag)) break;
            }
            STAMP(10);
            for (u32 sp = 0; sp < SPIN_CAP; ++sp) {
                xg = LD_C2(rsW, WS_X1 + col * 8);
                FENCE();
                if (all_ok(xg.y == tag)) break;
            }
            STAMP(11);
        }
        float a0 = lo16(xg.x), a1 = hi16(xg.x);
        ln_norm(lds, w, l, a0, a1);
        if (w < 7) g1_block(lds, w, l, pk(rb(a0 * rb(1.f + lo16(cb.x))) + lo16(sb.x), rb(a1 * rb(1.f + hi16(cb.x))) + hi16(sb.x)), B);
        BAR();
        const bool dead = l >= 56;
        const float* pp = (const float*)(lds + L_P1 + (dead ? 0 : (l >> 3)) * 512 + (w * 8 + ((l >> 1) & 3) * 2) * 8 + (l & 1) * 4);
        const float h0 = rb(sum_hi3(dead ? 0.f : pp[0])), h1 = rb(sum_hi3(dead ? 0.f : pp[2]));
        P = pk(silu(h0), silu(h1));
    }
    STAMP(12);
    {
        const u32 Pr = dppu<ROR4>(P);
        const u32 lo = (l & 4) ? Pr : P, hi = (l & 4) ? P : Pr;
        const v4u A = {dppu<0x44>(lo), dppu<0xEE>(lo), dppu<0x44>(hi), dppu<0xEE>(hi)};
        const bool on = ((l >> 2) & 3) == (l >> 4);
        const u32 m0 = (on && !(l & 2)) ? ~0u : 0u, m1 = (on && (l & 2)) ? ~0u : 0u;
        const v4u A0 = A & m0, A1 = A & m1;
        char* gp = lds + L_G2 + w * 4608 + l * 16;
#pragma unroll
        for (int jl = 0; jl < 4; ++jl) {
            v4f a = mfma(A0, BG[2 * jl], v4f{0, 0, 0, 0});
            a = mfma(A1, BG[2 * jl + 1], a);
            *(v4f*)(gp + jl * 1152) = a;
        }
    }
    BAR();
    if (w < 7) {
        const char* gp = lds + L_G2 + ((l & 7) >> 1) * 1152 + (w * 8 + (l >> 3)) * 16 + (l & 1) * 8;
        v2f a = *(const v2f*)gp;
#pragma unroll
        for (int i = 1; i < 8; ++i) a += *(const v2f*)(gp + i * 4608);
        ST_C4((v4u{asu(a.x), tag, asu(a.y), tag}), rsW, (isQ ? WS_WP + qn * 7168 : WS_FP + fu * 7168) + col * 16);
    }
    asm volatile(".Ladaln_e1:");
    STAMP(13);
    DUMP();
}

extern "C" void kernel_launch(void* const* d_in, const int* in_sizes, int n_in, void* d_out, int out_size, void* d_ws, size_t ws_size,
                              hipStream_t stream) {
    adaln_block_kernel<<<256, 512, 0, stream>>>((const char*)d_in[0], (const char*)d_in[1], (const char*)d_in[2], (const char*)d_in[3],
                                                (const char*)d_in[4], (const char*)d_in[6], (const char*)d_in[7], (const char*)d_in[8],
                                                (const char*)d_in[9], (char*)d_out, (char*)d_ws, (const u32*)d_ws);
}
```

```cpp
#include <hip/hip_runtime.h>

typedef unsigned u32;
typedef unsigned long long u64;
typedef unsigned short u16;
typedef u32 v4u __attribute__((ext_vector_type(4)));
typedef u32 v2u __attribute__((ext_vector_type(2)));
typedef float v4f __attribute__((ext_vector_type(4)));
typedef float v2f __attribute__((ext_vector_type(2)));
typedef u32 v8u __attribute__((ext_vector_type(8)));
typedef u32 v16u __attribute__((ext_vector_type(16)));
typedef short v4s __attribute__((ext_vector_type(4)));
typedef __bf16 v8bf __attribute__((ext_vector_type(8)));
typedef __bf16 v2bf __attribute__((ext_vector_type(2)));
typedef __amdgpu_buffer_rsrc_t rsrc_t;

#define WIN 5
#define ESZ 0x14151515u
#define LSZ 0x12111111u
#define QSZ 0x0d0e0e0eu
#define QSLEEP 10
#define QPOS 6
#define W0 2
#define W1 3
#define PFWG 4
#define DRY 0

constexpr u32 WS_MOD = 4096;
constexpr u32 WS_Q = 32768;
constexpr u32 WS_ST = 65536;
constexpr u32 WS_X1 = 73728;
constexpr u32 WS_WP = 81920;
constexpr u32 WS_FP = 311296;
constexpr u32 WS_O = 524288;
constexpr u32 WS_END = 1048576;
constexpr u32 OOB = 0x40000000u;
#define SPIN_CAP_V 400000u
constexpr u32 SPIN_CAP = SPIN_CAP_V;

constexpr int L_FLG = 0;
constexpr int L_ML = 64;
constexpr int L_F = L_ML + 128;
constexpr int L_ST = L_F + 64;
constexpr int L_QS = L_ST + 256;
constexpr int L_PW = L_QS + 1024;
constexpr int L_RR = L_PW + 1792;
constexpr int L_P1 = L_RR + 1024;
constexpr int L_OP = L_P1 + 7168;
constexpr int L_END = L_OP + 14336;

#define RS(p, n) __builtin_amdgcn_make_buffer_rsrc((void*)(p), (short)0, (int)(n), 0x00020000)
#define LD_NT(rs, vo, so) __builtin_amdgcn_raw_buffer_load_b128(rs, (int)(vo), (int)(so), 2)
#define LD_C4(rs, vo) __builtin_amdgcn_raw_buffer_load_b128(rs, (int)(vo), 0, 16)
#define LD_C2(rs, vo) __builtin_amdgcn_raw_buffer_load_b64(rs, (int)(vo), 0, 16)
#define ST_C4(v, rs, vo) __builtin_amdgcn_raw_buffer_store_b128(v, rs, (int)(vo), 0, 16)
#define ST_C2(v, rs, vo) __builtin_amdgcn_raw_buffer_store_b64(v, rs, (int)(vo), 0, 16)
#define FENCE() asm volatile("" ::: "memory")
#define ARG(v, i) ((char*)(((u64)v[2 * (i) + 1] << 32) | v[2 * (i)]))
#define kc ARG(ka, 0)
#define wq ARG(ka, 1)
#define wmod ARG(ka, 2)
#define cin ARG(ka, 3)
#define xin ARG(ka, 4)
#define ws ARG(ka, 5)
#define vc ARG(ka, 6)
#define wo ARG(ka, 7)
#define w1 ARG(kb, 0)
#define w2 ARG(kb, 1)
#define out ARG(kb, 2)
#define EXIT() asm volatile("s_endpgm" ::: "memory")
#define KEEP(x) asm volatile("" : "+v"(x))
#define KEEPS(x) asm volatile("" : "+s"(x))
#define KEEP8(x) asm volatile("" : "+v"(x[0]), "+v"(x[1]), "+v"(x[2]), "+v"(x[3]), "+v"(x[4]), "+v"(x[5]), "+v"(x[6]), "+v"(x[7]))
#define ANY8(x) asm volatile("" : "=v"(x[0]), "=v"(x[1]), "=v"(x[2]), "=v"(x[3]), "=v"(x[4]), "=v"(x[5]), "=v"(x[6]), "=v"(x[7]))
__device__ __forceinline__ bool spin_more(u32& n) { asm volatile("" : "+s"(n)); return --n != 0; }
#define SPIN(n) _Pragma("unroll 1") for (u32 sp_ = (n), go_ = 1; go_; go_ = spin_more(sp_))
#define NONE(bad) (__builtin_amdgcn_ballot_w64((bad) != 0u) == 0ull)
#define PIN() __builtin_amdgcn_sched_barrier(0)
#define LDSWAIT() asm volatile("s_waitcnt lgkmcnt(0)" ::: "memory")
#define BAR() do { asm volatile("s_waitcnt lgkmcnt(0)\n\ts_barrier" ::: "memory"); } while (0)
#define WINWAIT() asm volatile("s_waitcnt vmcnt(%0)" :: "i"(WIN - 1) : "memory")

__device__ __forceinline__ float asf(u32 x) { return __builtin_bit_cast(float, x); }
__device__ __forceinline__ u32 asu(float x) { return __builtin_bit_cast(u32, x); }
__device__ __forceinline__ float lo16(u32 x) { return asf(x << 16); }
__device__ __forceinline__ float hi16(u32 x) { return asf(x & 0xffff0000u); }
__device__ __forceinline__ u32 pk(float a, float b) { v2bf v = {(__bf16)a, (__bf16)b}; return __builtin_bit_cast(u32, v); }
__device__ __forceinline__ float rb(float a) { return (float)(__bf16)a; }
__device__ __forceinline__ void rb2(float& a, float& b) { const u32 t = pk(a, b); a = lo16(t); b = hi16(t); }
template <int C> __device__ __forceinline__ float dpp(float x) { return asf((u32)__builtin_amdgcn_update_dpp(0, (int)asu(x), C, 0xf, 0xf, true)); }
template <int C> __device__ __forceinline__ u32 dppu(u32 x) { return (u32)__builtin_amdgcn_update_dpp(0, (int)x, C, 0xf, 0xf, true); }
constexpr int QX1 = 0xB1, QX2 = 0x4E, HMIR = 0x141, ROR1 = 0x121, ROR2 = 0x122, ROR4 = 0x124, ROR8 = 0x128;
__device__ __forceinline__ float t32_add(float a, float b) { auto s = __builtin_amdgcn_permlane32_swap(asu(a), asu(b), false, false); return asf(s[0]) + asf(s[1]); }
__device__ __forceinline__ float t16_add(float a, float b) { auto s = __builtin_amdgcn_permlane16_swap(asu(a), asu(b), false, false); return asf(s[0]) + asf(s[1]); }
__device__ __forceinline__ float x16_max(float x) { auto s = __builtin_amdgcn_permlane16_swap(asu(x), asu(x), false, false); return fmaxf(asf(s[0]), asf(s[1])); }
__device__ __forceinline__ float x32_max(float x) { auto s = __builtin_amdgcn_permlane32_swap(asu(x), asu(x), false, false); return fmaxf(asf(s[0]), asf(s[1])); }
__device__ __forceinline__ float rows4(float a0, float a1, float a2, float a3) { return t16_add(t32_add(a0, a2), t32_add(a1, a3)); }
__device__ __forceinline__ float sum_lo3(float x) { x += dpp<QX1>(x); x += dpp<QX2>(x); x += dpp<HMIR>(x); return x; }
__device__ __forceinline__ float max_lo3(float x) { x = fmaxf(x, dpp<QX1>(x)); x = fmaxf(x, dpp<QX2>(x)); x = fmaxf(x, dpp<HMIR>(x)); return x; }
__device__ __forceinline__ float sum_row(float x) { x = sum_lo3(x); return x + dpp<ROR8>(x); }
__device__ __forceinline__ float max_row(float x) { x = max_lo3(x); return fmaxf(x, dpp<ROR8>(x)); }
__device__ __forceinline__ v4f mfma(v4u a, v4u b, v4f c) {
    return __builtin_amdgcn_mfma_f32_16x16x32_bf16(__builtin_bit_cast(v8bf, a), __builtin_bit_cast(v8bf, b), c, 0, 0, 0);
}
__device__ __forceinline__ v4f mfma16(v2u a, v2u b, v4f c) {
    return __builtin_amdgcn_mfma_f32_16x16x16bf16_1k(__builtin_bit_cast(v4s, a), __builtin_bit_cast(v4s, b), c, 0, 0, 0);
}
__device__ __forceinline__ bool all_ok(bool ok) { return __builtin_amdgcn_ballot_w64(!ok) == 0ull; }
__device__ __forceinline__ float silu(float v) { return v * __builtin_amdgcn_rcpf(1.f + __builtin_amdgcn_exp2f(v * -1.44269504089f)); }
__device__ __forceinline__ float rdl(float v, int lane) { return asf((u32)__builtin_amdgcn_readlane((int)asu(v), lane)); }
constexpr float SC = 0.0625f * 1.44269504089f;

__device__ __forceinline__ void tr8(const v4u* r, v4u* o) {
#pragma unroll
    for (int j = 0; j < 8; ++j) {
        const u32 sel = (j & 1) ? 0x07060302u : 0x05040100u;
#pragma unroll
        for (int t = 0; t < 4; ++t) o[j][t] = __builtin_amdgcn_perm(r[2 * t + 1][j >> 1], r[2 * t][j >> 1], sel);
    }
}
__device__ __forceinline__ v4u seal(v4f a, u32 tag) {
    return v4u{__builtin_amdgcn_perm(asu(a[0]), tag, 0x07060500u), __builtin_amdgcn_perm(asu(a[1]), tag, 0x07060501u),
               __builtin_amdgcn_perm(asu(a[2]), tag, 0x07060502u), __builtin_amdgcn_perm(asu(a[3]), tag, 0x07060503u)};
}
__device__ __forceinline__ v4u tag_bytes(u32 t) { return v4u{t & 255u, (t >> 8) & 255u, (t >> 16) & 255u, t >> 24}; }
__device__ __forceinline__ v4u poll16(rsrc_t rs, u32 off, v4u tb) {
    v4u y;
    SPIN(SPIN_CAP) {
        y = LD_C4(rs, off) ^ tb;
        FENCE();
        if (NONE((y.x | y.y | y.z | y.w) & 255u)) break;
    }
    return y;
}

#define STAMP(i)
#define DUMP()

__device__ __forceinline__ void ln_part(char* lds, int w, int l, float a0, float a1) {
    const float K0 = asf((u32)__builtin_amdgcn_readfirstlane((int)asu(a0))), K1 = asf((u32)__builtin_amdgcn_readfirstlane((int)asu(a1)));
    const float d0 = a0 - K0, d1 = a1 - K1;
    float u = sum_row(rows4(d0, d1, d0 * d0, d1 * d1));
    const bool isk = (l & 47) == 1;
    u = isk ? ((l & 16) ? K1 : K0) : u;
    if ((l & 15) == 0 || isk) *(float*)(lds + L_ST + ((l >> 4) & 1) * 128 + w * 16 + (isk ? 8 : (l >> 5) * 4)) = u;
}
__device__ __forceinline__ void ln_fin(char* lds, int l, float& a0, float& a1) {
    const v4f s = *(const v4f*)(lds + L_ST + (l & 15) * 16);
    const bool live = (l & 7) != 7;
    const float M = sum_lo3(live ? s.x + 64.f * s.z : 0.f) * (1.f / 448);
    const float e = M - s.z;
    const float V = sum_lo3(live ? s.y + e * (64.f * e - 2.f * s.x) : 0.f) * (1.f / 448);
    const float r = __builtin_amdgcn_rcpf(rb(__builtin_amdgcn_sqrtf(rb(rb(V) + 1e-5f)))), Mb = rb(M);
    a0 -= rdl(Mb, 0); a1 -= rdl(Mb, 8);
    rb2(a0, a1);
    a0 *= rdl(r, 0); a1 *= rdl(r, 8);
    rb2(a0, a1);
}

template <int QD, int JJ> __device__ __forceinline__ u32 bank_mov(u32 old, u32 P) {
    constexpr int C = QD == JJ ? 0xE4 : QD > JJ ? 0x110 + 4 * (QD - JJ) : 0x100 + 4 * (JJ - QD);
    return (u32)__builtin_amdgcn_update_dpp((int)old, (int)P, C, 0xf, 1 << QD, false);
}
template <int QD> __device__ __forceinline__ void bank4(v4u& A, u32 P) {
    A[0] = bank_mov<QD, 0>(A[0], P); A[1] = bank_mov<QD, 1>(A[1], P); A[2] = bank_mov<QD, 2>(A[2], P); A[3] = bank_mov<QD, 3>(A[3], P);
}
__device__ __forceinline__ void build_a(u32 hv, u32 psel, v4u* A) {
    const u32 P = __builtin_amdgcn_perm(dppu<QX1>(hv), hv, psel);
    bank4<0>(A[0], P); bank4<1>(A[1], P); bank4<2>(A[2], P); bank4<3>(A[3], P);
}
__device__ __forceinline__ void zero_a(v4u* A) {
#pragma unroll
    for (int i = 0; i < 4; ++i) { A[i] = v4u{0, 0, 0, 0}; asm volatile("" : "+v"(A[i])); }
}
__device__ __forceinline__ v4f gemv(const v4u* A, const v4u* B) {
    v4f a0 = {0, 0, 0, 0}, a1 = {0, 0, 0, 0};
#pragma unroll
    for (int qd = 0; qd < 4; ++qd) {
        a0 = mfma(A[qd], B[2 * qd], a0);
        a1 = mfma(A[qd], B[2 * qd + 1], a1);
    }
    return v4f{a0[0] + dpp<ROR8>(a0[2]), a0[1] + dpp<ROR8>(a0[3]), a1[0] + dpp<ROR8>(a1[2]), a1[1] + dpp<ROR8>(a1[3])};
}
__device__ __forceinline__ u32 wvo(int l, u32 pitch) { return ((l >> 4) * 16 + ((l >> 3) & 1) * 2) * pitch + (l & 7) * 16; }
__device__ __forceinline__ v2f sum7(const char* p) {
    v2f s = *(const v2f*)p;
#pragma unroll
    for (int i = 1; i < 7; ++i) s += *(const v2f*)(p + i * 512);
    return s;
}

__global__ void __launch_bounds__(512, 2)
adaln_block_kernel(const char*, const char*, const char*, const char*, const char*, char*, const char*, const char*,
                   const char*, const char*, char*, char*) {
    __shared__ __attribute__((aligned(16))) char lds[L_END];
    STAMP(0);
    v16u ka;
    v8u kb;
    u32 bid = blockIdx.x, tid = threadIdx.x;
    asm volatile("s_load_dwordx16 %0, %3, 0x0\n\ts_load_dwordx8 %1, %3, 0x40"
                 : "=&s"(ka), "=&s"(kb), "+s"(bid) : "s"(__builtin_amdgcn_kernarg_segment_ptr()));
    asm volatile("" : "+v"(tid));
    u32 pfv = 0;
    {
        u64 pc; u32 len;
        asm volatile("s_getpc_b64 %0\n.Ladaln_pc:\n\ts_max_i32 %1, .Ladaln_e0-.Ladaln_pc, 0\n\ts_max_i32 %1, .Ladaln_e1-.Ladaln_pc, %1\n\t"
                     "s_max_i32 %1, .Ladaln_e2-.Ladaln_pc, %1" : "=s"(pc), "=s"(len) :: "scc");
        const rsrc_t rsc = RS(pc, bid < PFWG * 8 ? len : 0);
        pfv = __builtin_amdgcn_raw_buffer_load_b32(rsc, tid * 128, 0, 0);
        asm volatile("" : "+s"(bid) :: "memory");
    }
    const u32 late4 = bid & 4, idx = bid >> 3, xc = bid & 7;
    const int rel = idx + late4 - 4;
    const bool isX = rel < 0, isQ = rel < 8;
    u32 ng = (u32)((((u64)LSZ << 32) | ESZ) >> (xc * 8)) & 255u;
    u32 g0 = ((idx >> 2) + (late4 >> 1) - 3) * ng;
    g0 = late4 ? 256 - g0 : g0;
    u32 slab = ((bid >> 1) & 12) | (bid & 3);
    u32 qn = xc * 8 + idx;
    KEEPS(qn);
    qn += late4 - 4;
    u32 split = (late4 ? 3 : -2) + (rel >> 2);
    {
        const u32 sh = (xc & 3) * 8, e5 = ((ESZ >> sh) & 255) * 5, qe = (QSZ >> sh) & 255, rem = 256 - e5 - ((LSZ >> sh) & 255) * 6 - 2 * qe, hi = (rel >> 2) & 1;
        const u32 qg = e5 + (late4 ? 2 * qe + (hi ? rem >> 1 : 0) : hi * qe), qk = late4 ? (hi ? rem - (rem >> 1) : rem >> 1) : qe;
        g0 = isQ ? qg : g0;
        ng = isQ ? qk : ng;
        split = isQ ? 11 + (late4 >> 1) + hi : split;
    }
    u32 kvb = ((slab << 10) + 0x300 + g0) << 12, kvn = ng << 12;
    int fu = (rel - 8) * 8 + xc;
    const bool isF = !isQ && fu < 28;
    const int w = __builtin_amdgcn_readfirstlane(tid >> 6);
    const int l = tid & 63, col = tid;
    u32 psel = (l & 1) ? 0x03020706u : 0x05040100u;
    const int gpos = ((l & 7) * 8 + (l >> 4) * 2) * 8;
    u32 kvo = (tid & 7) * 512 + (tid & 8) * 8 + (tid & 48);
    u32 oa = WS_O + ((slab * 2 + (w & 1)) * 16 + split) * 1024 + l * 16;
    u32 ra = L_OP + (w & 1) * 1024 + l * 8;
    const u32 pitch = isX ? 5376u : 8192u;
    u32 n0 = isQ ? pitch * 448u : kvn, nx = isQ ? 1792u : 0u, ko = isQ ? (isX ? slab : qn) << 7 : kvb;
    u32 s1 = isQ ? pitch : 128u, s2 = isQ ? pitch * 4 : 256u, s3 = s1 + s2;
    u32 vo = isQ ? ((tid & 0x1f0) | ((tid >> 2) & 2)) * pitch + (tid & 7) * 16 : (tid >> 6) * 4096 + kvo;
    u32 vo2 = vo + (isQ ? pitch * 8 : 28672u), xo2 = tid * 2;
    asm volatile("" : "+v"(vo), "+v"(vo2), "+v"(xo2), "+v"(kvo), "+v"(oa), "+v"(ra), "+v"(psel));
    asm volatile("" : "+s"(kvb), "+s"(kvn), "+s"(ng), "+s"(split), "+s"(slab), "+s"(qn), "+s"(fu));
    STAMP(3);
    asm volatile("s_waitcnt lgkmcnt(0)" : "+s"(ka), "+s"(kb), "+s"(n0), "+s"(nx), "+s"(ko), "+s"(s1), "+s"(s2), "+s"(s3));
    STAMP(4);
    u32 tag = *(const __attribute__((address_space(4))) u32*)(u64)ws;
#define GET_TAG() do { KEEPS(tag); tag = tag * 1664525u + 1013904223u; } while (0)
    const rsrc_t rsW = RS(ws, WS_END);
    v4u G[8];

    if (__builtin_expect(w < 7, 1)) {
        v4u R[8];
        u32 xi0, xi1;
        {
            const rsrc_t rsv = RS(isX ? cin : xin, nx);
            xi0 = __builtin_amdgcn_raw_buffer_load_b16(rsv, xo2, 0, 0);
            xi1 = __builtin_amdgcn_raw_buffer_load_b16(rsv, xo2 + 896, 0, 0);
            const char* fp = isX ? wmod : wq;
            const rsrc_t rs0 = RS((isQ ? fp : kc) + ko, n0);
#define LD0(i) R[i] = LD_NT(rs0, (i) < 4 ? vo : vo2, ((i) & 3) == 0 ? 0u : ((i) & 3) == 1 ? s1 : ((i) & 3) == 2 ? s2 : s3)
#pragma unroll
            for (int i = 0; i < W0; ++i) LD0(i);
            FENCE();
            if (!isQ) asm volatile("s_waitcnt vmcnt(%0)" :: "i"(W0 - 1) : "memory");
#pragma unroll
            for (int i = W0; i < W0 + W1; ++i) LD0(i);
            FENCE();
            if (!isQ) asm volatile("s_waitcnt vmcnt(%0)" :: "i"(W1) : "memory");
#pragma unroll
            for (int i = W0 + W1; i < 8; ++i) LD0(i);
            FENCE();
            PIN();
        }
        if (isX) {
            if (slab >= 14) EXIT();
            const rsrc_t rs0 = RS(wmod, 448 * 5376);
            u32 so = slab * 128;
            v4u B[8], A[4];
            zero_a(A);
            build_a(pk(silu(lo16(xi0)), silu(lo16(xi1))), psel, A);
#pragma unroll 1
            for (int u = 0; u < 3; ++u) {
                tr8(R, B);
                KEEP8(B);
                if (u == 0) STAMP(2);
                const v4f t = gemv(A, B);
                if (!(l & 8)) *(v4f*)(lds + L_P1 + (u & 1) * 3584 + w * 512 + gpos) = t;
                BAR();
                so = u == 2 ? OOB : so + 14 * 128;
#pragma unroll
                for (int i = 0; i < 8; ++i) R[i] = LD_NT(rs0, vo, so + ((i >> 1) * 4 + (i & 1)) * 5376);
                FENCE();
            }
            asm volatile(".Ladaln_e2:");
            DUMP();
            EXIT();
        }
        GET_TAG();
        v4u K2[4], V[12];
        const rsrc_t rsK = RS(kc + kvb, kvn), rsV = RS(vc + kvb, kvn);
        u32 sl[3] = {w * 4096u, w * 4096u + 28672u, w * 4096u + 57344u};
        asm volatile("" : "+s"(sl[0]), "+s"(sl[1]), "+s"(sl[2]));
#define LDK(i) LD_NT(rsK, kvo + ((i) & 3) * 128, sl[(i) >> 2])
#define LDV(i) V[i] = LD_NT(rsV, l * 16 + ((i) & 3) * 1024, sl[(i) >> 2])
#define LDG(i) G[i] = LD_NT(rsG, ((i) & 1) ? gv1 : gv0, gs[(i) >> 1])
        char* pw = lds + L_PW + w * 256;
        if (l < 32) *(u32*)(pw + 128 + l * 4) = 0;
        float a0 = lo16(xi0), a1 = lo16(xi1);
        if (isQ) ln_part(lds, w, l, a0, a1);
        BAR();
        if (isQ) {
            ln_fin(lds, l, a0, a1);
            KEEP(a0); KEEP(a1);
            v4u B[8], A[4];
            zero_a(A);
            tr8(R, B);
            KEEP8(B);
            PIN();
            STAMP(1);
            v2u sa, ca;
            SPIN(SPIN_CAP) {
                sa = LD_C2(rsW, WS_MOD + col * 8);
                ca = LD_C2(rsW, WS_MOD + 3584 + col * 8);
                FENCE();
                if (NONE((sa.y ^ tag) | (ca.y ^ tag))) break;
            }
            STAMP(2);
            build_a(pk(rb(a0 * rb(1.f + lo16(ca.x))) + lo16(sa.x), rb(a1 * rb(1.f + hi16(ca.x))) + hi16(sa.x)), psel, A);
            const v4f t = gemv(A, B);
            if (!(l & 8)) *(v4f*)(lds + L_P1 + w * 512 + gpos) = t;
            BAR();
#pragma unroll
            for (int i = 0; i < 8; ++i) R[i] = LDK(i);
            FENCE(); PIN();
        }
#pragma unroll
        for (int i = 0; i < 4; ++i) { WINWAIT(); K2[i] = LDK(8 + i); }
        const int row = l & 15, kq = l >> 4;
        const rsrc_t rsG = RS(isQ ? wo + (size_t)((qn >> 2) * 256 + (qn & 3) * 2) * 896 : w2 + (size_t)(isF ? fu : 0) * 57344, isQ ? 250 * 896 : isF ? 57344 : 0);
        u32 gv0 = (isQ ? kq * 32 + (row >> 3) : kq * 16 + (row >> 3) * 2) * 896 + (l & 7) * 16, gv1 = gv0 + (isQ ? 128 * 896 : 896);
        const u32 gst = isQ ? 7168u : 3584u;
        u32 gs[4] = {w * 128u, w * 128u + gst, w * 128u + 2 * gst, w * 128u + 3 * gst};
        u32 wa = L_OP + w * 2048 + l * 8;
        asm volatile("" : "+v"(gv0), "+v"(gv1), "+v"(wa));
        asm volatile("" : "+s"(gs[0]), "+s"(gs[1]), "+s"(gs[2]), "+s"(gs[3]));
        float s = 0;
        v4u PA[4];
        v2u PB[4];
#pragma unroll
        for (int i = 0; i < 12; ++i) {
            WINWAIT(); LDV(i);
            FENCE(); PIN();
            if (i == QPOS) {
                for (;;) {
                    FENCE();
                    if (__builtin_amdgcn_readfirstlane(*(const u32*)(lds + L_FLG))) break;
                    __builtin_amdgcn_s_sleep(1);
                }
                FENCE();
                STAMP(6);
                const char* qb = lds + L_QS + (row & 1) * 512 + ((row >> 1) & 1) * 64 + kq * 16;
                v4f sc[3];
#pragma unroll
                for (int g = 0; g < 3; ++g) sc[g] = v4f{0, 0, 0, 0};
#pragma unroll
                for (int j = 0; j < 4; ++j) {
                    const v4u A = *(const v4u*)(qb + j * 128);
#pragma unroll
                    for (int g = 0; g < 3; ++g) sc[g] = mfma(A, g < 2 ? R[g * 4 + j] : K2[j], sc[g]);
                }
                const v4f sg = kq == 0 ? sc[0] : kq == 1 ? sc[1] : sc[2];
                s = (row < 8 ? sg[0] : sg[3]) + dpp<ROR8>(row < 8 ? sg[1] : sg[2]);
                s = (kq < 3 && kq * 7 + w < (int)ng) ? rb(s) : -INFINITY;
                PIN();
            }
            if (i == QPOS + 1) {
                const float m = x32_max(x16_max(max_lo3(s)));
                const float p = __builtin_amdgcn_exp2f((s - (m == -INFINITY ? 0.f : m)) * SC);
                *(u16*)(pw + (kq >> 1) * 64 + (l & 1) * 32 + ((l >> 3) & 1) * 16 + ((kq & 1) * 4 + ((l >> 1) & 3)) * 2) = (u16)pk(p, p);
                float ls = sum_lo3(p);
                ls = t16_add(ls, ls);
                ls = t32_add(ls, ls);
                if ((l & 55) == 0) *(v2f*)(lds + L_ML + (l + w) * 8) = v2f{m, ls};
                PIN();
            }
            if (i == QPOS + 2) {
                LDSWAIT();
                const int real = (kq >> 1) * 32 + ((row >> 1) & 1) * 16;
                const bool on = (kq & 1) == (row & 1);
#pragma unroll
                for (int qd = 0; qd < 4; ++qd) {
                    const char* ap = pw + ((on && (row >> 2) == qd) ? real : 128);
                    PA[qd] = *(const v4u*)ap;
                    PB[qd] = *(const v2u*)(ap + 64);
                }
                PIN();
            }
        }
        STAMP(5);
        WINWAIT(); LDG(0);
        FENCE(); PIN();
        v4f o0 = {0, 0, 0, 0}, o1 = {0, 0, 0, 0};
        {
            v4u BV[8];
            tr8(V, BV);
#pragma unroll
            for (int qd = 0; qd < 4; ++qd) {
                o0 = mfma(PA[qd], BV[2 * qd], o0);
                o1 = mfma(PA[qd], BV[2 * qd + 1], o1);
            }
        }
        KEEP(o0); KEEP(o1);
        PIN();
        STAMP(7);
#pragma unroll
        for (int i = 1; i < 3; ++i) { WINWAIT(); LDG(i); }
        FENCE(); PIN();
        {
            v2u BV[8];
#pragma unroll
            for (int j = 0; j < 8; ++j) BV[j].x = __builtin_amdgcn_perm(V[9][j >> 1], V[8][j >> 1], (j & 1) ? 0x07060302u : 0x05040100u);
            PIN();
            WINWAIT();
#pragma unroll
            for (int i = 3; i < 8; ++i) LDG(i);
            FENCE(); PIN();
#pragma unroll
            for (int j = 0; j < 8; ++j) BV[j].y = __builtin_amdgcn_perm(V[11][j >> 1], V[10][j >> 1], (j & 1) ? 0x07060302u : 0x05040100u);
#pragma unroll
            for (int qd = 0; qd < 4; ++qd) {
                o0 = mfma16(PB[qd], BV[2 * qd], o0);
                o1 = mfma16(PB[qd], BV[2 * qd + 1], o1);
            }
        }
        *(v2f*)(lds + wa) = v2f{o0[0], o0[1]};
        *(v2f*)(lds + wa + 1024) = v2f{o0[2], o0[3]};
        *(v2f*)(lds + wa + 512) = v2f{o1[0], o1[1]};
        *(v2f*)(lds + wa + 1536) = v2f{o1[2], o1[3]};
        FENCE();
    } else {
        GET_TAG();
        if (isX) {
            if (slab >= 14) EXIT();
#pragma unroll 1
            for (int u = DRY ? -1 : 0; u < 3; ++u) {
                if (u >= 0) BAR();
                const v2f s = sum7(lds + L_P1 + (u & 1) * 3584 + l * 8);
                ST_C2((v2u{pk(s.x, s.y), tag}), rsW, u < 0 ? OOB : WS_MOD + ((slab + u * 14) * 64 + l) * 8);
                if (u == 0) STAMP(3); else STAMP(4);
            }
            DUMP();
            EXIT();
        }
        if (l < 16) *(v2u*)(lds + L_ML + l * 8) = (l & 7) == 7 ? v2u{0xff800000u, 0u} : v2u{~0u, ~0u};
        if (l == 0) *(u32*)(lds + L_FLG) = 0;
        BAR();
        if (isQ) {
            BAR();
            const v2f s = sum7(lds + L_P1 + l * 8);
            const u32 hv = pk(s.x, s.y);
            ST_C2((v2u{__builtin_amdgcn_perm(dppu<QX1>(hv), hv, psel), tag}), rsW, WS_Q + (l & 1) * 16384 + (qn * 32 + (l >> 1)) * 8);
            STAMP(3);
        } else __builtin_amdgcn_s_sleep(QSLEEP);
        v4u qa, qb;
        const u32 qo = WS_Q + slab * 2048 + l * 16;
        SPIN(SPIN_CAP) {
            qa = LD_C4(rsW, qo);
            qb = LD_C4(rsW, qo + 1024);
            FENCE();
            if (NONE((qa.y ^ tag) | (qa.w ^ tag) | (qb.y ^ tag) | (qb.w ^ tag))) break;
        }
        *(v2u*)(lds + L_QS + l * 8) = v2u{qa.x, qa.z};
        *(v2u*)(lds + L_QS + 512 + l * 8) = v2u{qb.x, qb.z};
        LDSWAIT();
        *(u32*)(lds + L_FLG) = 1;
        FENCE();
        STAMP(5);
        v2u ml;
        SPIN(SPIN_CAP * 16) {
            FENCE();
            ml = *(const v2u*)(lds + L_ML + (l & 15) * 8);
            if (all_ok(ml.x != ~0u && ml.y != ~0u)) break;
            __builtin_amdgcn_s_sleep(1);
        }
        const float M = max_lo3(asf(ml.x));
        const float f = __builtin_amdgcn_exp2f((asf(ml.x) - M) * SC);
        const float L = sum_lo3(f * asf(ml.y));
        if (l < 16) *(float*)(lds + L_F + l * 4) = f;
        if ((l & 55) == 0) ST_C4((v4u{asu(M), tag, asu(L), tag}), rsW, WS_ST + ((slab * 2 + (l >> 3)) * 16 + split) * 16);
        STAMP(7);
        asm volatile(".Ladaln_e1:");
        ANY8(G);
    }
#pragma unroll 1
    for (int it = (DRY && w == 7) ? 0 : 1; it < 2; ++it) {
        if (it) BAR();
        if (w >= 6) {
            const char* fp = lds + L_F + (w & 1) * 32;
            v4f fa = *(const v4f*)fp, fb = *(const v4f*)(fp + 16);
            v2f x[7], y[7];
#pragma unroll
            for (int i = 0; i < 7; ++i) { x[i] = *(const v2f*)(lds + ra + i * 2048); y[i] = *(const v2f*)(lds + ra + i * 2048 + 512); }
            PIN();
            v2f ax = x[0] * fa[0], ay = y[0] * fa[0];
#pragma unroll
            for (int i = 1; i < 7; ++i) { const float f = i < 4 ? fa[i] : fb[i - 4]; ax += x[i] * f; ay += y[i] * f; }
            ST_C4(seal(v4f{ax.x, ax.y, ay.x, ay.y}, tag), rsW, it ? oa : OOB);
        }
    }
    STAMP(9);
    if (!isQ && !isF) { DUMP(); EXIT(); }

    v4u BG[8], B[8], A[4], R[8];
    zero_a(A);
    ANY8(BG); ANY8(B); ANY8(R);
    u32 rel4 = idx + late4;
    KEEPS(rel4);
    const bool onlyQ = rel4 < 12;
    const int hq = qn >> 2, sp4 = (w >> 1) * 4 + (l >> 4), bb = (l >> 3) & 1, c7 = fu * 16 + ((l & 31) >> 1);
    const u32 po = ((l >> 3) * 8 + w) * 3584 + fu * 128 + (l & 7) * 16;
    const u32 oo = sp4 == 15 ? OOB : WS_O + ((bb * 16 + hq) * 16 + sp4) * 1024 + (qn & 3) * 256 + (w & 1) * 128 + (l & 7) * 16;
    u32 off = isQ ? oo : WS_WP + po;
    v4u tb = tag_bytes((isQ && sp4 == 15) ? 0u : tag);
    char* wa = lds + (isQ ? L_P1 + (w >> 1) * 512 + ((w & 1) * 32 + (l & 7) * 4 + (l >> 4)) * 8 + bb * 4 : L_RR + ((l & 7) * 4 + (l >> 4)) * 32 + w * 4);
    const u32 go = (l & 8) ? OOB : (isQ ? WS_WP + qn * 3584 : WS_FP + fu * 3584) + w * 512 + gpos;
    const u32 mo0 = WS_MOD + (w < 7 ? 1344 + col : 896 + c7) * 8, mo1 = WS_MOD + (w < 7 ? 1792 + col : 2240 + c7) * 8;
    const u32 xo = (l & 1) * 896 + c7 * 2;
    const rsrc_t rso = RS(out, 1792);
    v2u ma = {0, 0}, mb = {0, 0};
    u32 xv = 0;
    if (isF) {
        ma = LD_C2(rsW, mo0);
        mb = LD_C2(rsW, mo1);
        xv = __builtin_amdgcn_raw_buffer_load_b16(RS(xin, 1792), xo, 0, 0);
        const rsrc_t rs1 = RS(w1, 448 * 3584);
        const u32 v1 = wvo(l, 3584);
#pragma unroll
        for (int i = 0; i < 8; ++i) R[i] = LD_NT(rs1, v1, w * 64 * 3584 + fu * 128 + ((i >> 1) * 4 + (i & 1)) * 3584);
        FENCE();
    }
    if (onlyQ && w < 3) *(v2f*)(lds + L_P1 + (4 + w) * 512 + l * 8) = v2f{0.f, 0.f};
    float fac = 1.f;
    float m0 = 0, m1 = 0, m2 = 0, m3 = 0;
#pragma unroll 1
    for (int ph = DRY ? 0 : 1; ph < 3; ++ph) {
        v4u y = {0, 0, 0, 0};
        if (ph == 1) {
            if (onlyQ) {
                const bool dead = (l & 15) == 15;
                const u32 tl = dead ? 0u : tag;
                v4u st;
                SPIN(SPIN_CAP) {
                    st = LD_C4(rsW, dead ? OOB : WS_ST + (((l & 16) + hq) * 16 + (l & 15)) * 16);
                    FENCE();
                    if (NONE((st.y ^ tl) | (st.w ^ tl))) break;
                }
                const float m = dead ? -INFINITY : asf(st.x);
                float f = __builtin_amdgcn_exp2f((m - max_row(m)) * SC);
                f *= __builtin_amdgcn_rcpf(sum_row(f * asf(st.z)));
                fac = asf((u32)__builtin_amdgcn_ds_bpermute((bb * 16 + sp4) * 4, (int)asu(f)));
            }
            if (isF) {
                SPIN(SPIN_CAP) {
                    if (NONE((ma.y ^ tag) | (mb.y ^ tag))) break;
                    ma = LD_C2(rsW, mo0);
                    mb = LD_C2(rsW, mo1);
                    FENCE();
                }
                asm volatile("" : "+v"(xv));
                const float ga = (l & 1) ? hi16(ma.x) : lo16(ma.x), gb = (l & 1) ? hi16(mb.x) : lo16(mb.x), c1 = rb(1.f + hi16(mb.x));
                m0 = lo16(w < 7 ? ma.x : xv); m1 = w < 7 ? hi16(ma.x) : ga; m2 = rb(1.f + lo16(mb.x)); m3 = w < 7 ? c1 : gb;
                KEEP8(R);
                tr8(R, B);
            }
            KEEP8(G);
            tr8(G, BG);
            KEEP(fac);
            PIN();
            STAMP(10);
        }
        if (ph) {
            y = poll16(rsW, off, tb);
            if (ph == 1) STAMP(11); else STAMP(14);
        }
        float t = rows4(asf(y.x) * fac, asf(y.y) * fac, asf(y.z) * fac, asf(y.w) * fac);
        const float t2 = t + dpp<ROR8>(t);
        *(float*)wa = isQ ? t : t2;
        BAR();
        if (w == 7 && isF) {
            const v4f q = *(const v4f*)(lds + L_RR + (l & 31) * 32 + (l >> 5) * 16);
            float s = (q[0] + q[1]) + (q[2] + q[3]);
            s = t32_add(s, s);
            const u32 yb = pk(m0 + rb(m1 * rb(s)), 0.f);
            ST_C2((v2u{yb | (dppu<QX1>(yb) << 16), tag}), rsW, (ph == 1 && !(l & 33)) ? WS_X1 + c7 * 8 : OOB);
            __builtin_amdgcn_raw_buffer_store_b16((u16)yb, rso, (ph == 2 && l < 32) ? xo : OOB, 0, 0);
            __builtin_amdgcn_raw_buffer_store_b32(tag, rsW, (ph == 2 && fu == 0 && l == 0) ? 0 : OOB, 0, 16);
            m0 = lo16(yb); m1 = m3;
            if (ph == 1) STAMP(12);
        }
        if (ph == 2) { STAMP(15); DUMP(); EXIT(); }
        if (isF) {
            float a0, a1;
            if (w < 7) {
                v2u xg = {0, 0};
                if (ph) {
                    SPIN(SPIN_CAP) {
                        xg = LD_C2(rsW, WS_X1 + col * 8);
                        FENCE();
                        if (NONE(xg.y ^ tag)) break;
                    }
                    STAMP(12);
                }
                a0 = lo16(xg.x); a1 = hi16(xg.x);
                ln_part(lds, w, l, a0, a1);
            }
            BAR();
            if (w < 7) {
                ln_fin(lds, l, a0, a1);
                a0 *= m2; a1 *= m3;
                rb2(a0, a1);
                build_a(pk(a0 + m0, a1 + m1), psel, A);
                const v4f t1 = gemv(A, B);
                if (!(l & 8)) *(v4f*)(lds + L_P1 + w * 512 + gpos) = t1;
            }
            BAR();
        }
        if (w < 7) {
            const v2f s = sum7(lds + L_P1 + l * 8);
            u32 hv = pk(s.x, s.y);
            if (isF) hv = pk(silu(lo16(hv)), silu(hi16(hv)));
            build_a(hv, psel, A);
            ST_C4(seal(gemv(A, BG), tag), rsW, ph ? go : OOB);
            if (ph) STAMP(13);
        }
        if (ph) {
            if (isQ) { DUMP(); EXIT(); }
            const bool dead = (l >> 3) * 8 + w >= 28;
            off = dead ? OOB : WS_FP + po;
            tb = tag_bytes(dead ? 0u : tag);
        }
    }
    asm volatile(".Ladaln_e0:" :: "v"(pfv));
}

extern "C" void kernel_launch(void* const* d_in, const int* in_sizes, int n_in, void* d_out, int out_size, void* d_ws, size_t ws_size,
                              hipStream_t stream) {
    adaln_block_kernel<<<256, 512, 0, stream>>>((const char*)d_in[1], (const char*)d_in[3], (const char*)d_in[9], (const char*)d_in[8],
                                                (const char*)d_in[0], (char*)d_ws, (const char*)d_in[2], (const char*)d_in[4],
                                                (const char*)d_in[6], (const char*)d_in[7], (char*)d_out, (char*)d_out);
}
```

```cpp
#define PROBE 3
#include <hip/hip_runtime.h>

typedef unsigned u32;
typedef unsigned long long u64;
typedef unsigned short u16;
typedef u32 v4u __attribute__((ext_vector_type(4)));
typedef u32 v2u __attribute__((ext_vector_type(2)));
typedef float v4f __attribute__((ext_vector_type(4)));
typedef float v2f __attribute__((ext_vector_type(2)));
typedef u32 v8u __attribute__((ext_vector_type(8)));
typedef u32 v16u __attribute__((ext_vector_type(16)));
typedef short v4s __attribute__((ext_vector_type(4)));
typedef __bf16 v8bf __attribute__((ext_vector_type(8)));
typedef __bf16 v2bf __attribute__((ext_vector_type(2)));
typedef __amdgpu_buffer_rsrc_t rsrc_t;

#define WIN 5
#define ESZ 0x14151515u
#define LSZ 0x12111111u
#define QSZ 0x0d0e0e0eu
#define QSLEEP 10
#define QPOS 6
#define W0 2
#define W1 3
#define PFWG 4
#define DRY 0

constexpr u32 WS_MOD = 4096;
constexpr u32 WS_Q = 32768;
constexpr u32 WS_ST = 65536;
constexpr u32 WS_X1 = 73728;
constexpr u32 WS_WP = 81920;
constexpr u32 WS_FP = 311296;
constexpr u32 WS_O = 524288;
constexpr u32 WS_END = 1048576;
constexpr u32 OOB = 0x40000000u;
#define SPIN_CAP_V 400000u
constexpr u32 SPIN_CAP = SPIN_CAP_V;

constexpr int L_FLG = 0;
constexpr int L_ML = 64;
constexpr int L_F = L_ML + 128;
constexpr int L_ST = L_F + 64;
constexpr int L_QS = L_ST + 256;
constexpr int L_PW = L_QS + 1024;
constexpr int L_RR = L_PW + 1792;
constexpr int L_P1 = L_RR + 1024;
constexpr int L_OP = L_P1 + 7168;
constexpr int L_END = L_OP + 14336;

#define RS(p, n) __builtin_amdgcn_make_buffer_rsrc((void*)(p), (short)0, (int)(n), 0x00020000)
#define LD_NT(rs, vo, so) __builtin_amdgcn_raw_buffer_load_b128(rs, (int)(vo), (int)(so), 2)
#define LD_C4(rs, vo) __builtin_amdgcn_raw_buffer_load_b128(rs, (int)(vo), 0, 16)
#define LD_C2(rs, vo) __builtin_amdgcn_raw_buffer_load_b64(rs, (int)(vo), 0, 16)
#define ST_C4(v, rs, vo) __builtin_amdgcn_raw_buffer_store_b128(v, rs, (int)(vo), 0, 16)
#define ST_C2(v, rs, vo) __builtin_amdgcn_raw_buffer_store_b64(v, rs, (int)(vo), 0, 16)
#define FENCE() asm volatile("" ::: "memory")
#define ARG(v, i) ((char*)(((u64)v[2 * (i) + 1] << 32) | v[2 * (i)]))
#define kc ARG(ka, 0)
#define wq ARG(ka, 1)
#define wmod ARG(ka, 2)
#define cin ARG(ka, 3)
#define xin ARG(ka, 4)
#define ws ARG(ka, 5)
#define vc ARG(ka, 6)
#define wo ARG(ka, 7)
#define w1 ARG(kb, 0)
#define w2 ARG(kb, 1)
#define out ARG(kb, 2)
#define EXIT() asm volatile("s_endpgm" ::: "memory")
#define KEEP(x) asm volatile("" : "+v"(x))
#define KEEPS(x) asm volatile("" : "+s"(x))
#define KEEP8(x) asm volatile("" : "+v"(x[0]), "+v"(x[1]), "+v"(x[2]), "+v"(x[3]), "+v"(x[4]), "+v"(x[5]), "+v"(x[6]), "+v"(x[7]))
#define ANY8(x) asm volatile("" : "=v"(x[0]), "=v"(x[1]), "=v"(x[2]), "=v"(x[3]), "=v"(x[4]), "=v"(x[5]), "=v"(x[6]), "=v"(x[7]))
__device__ __forceinline__ bool spin_more(u32& n) { asm volatile("" : "+s"(n)); return --n != 0; }
#define SPIN(n) _Pragma("unroll 1") for (u32 sp_ = (n), go_ = 1; go_; go_ = spin_more(sp_))
#define NONE(bad) (__builtin_amdgcn_ballot_w64((bad) != 0u) == 0ull)
#define PIN() __builtin_amdgcn_sched_barrier(0)
#define LDSWAIT() asm volatile("s_waitcnt lgkmcnt(0)" ::: "memory")
#define BAR() do { asm volatile("s_waitcnt lgkmcnt(0)\n\ts_barrier" ::: "memory"); } while (0)
#define WINWAIT() asm volatile("s_waitcnt vmcnt(%0)" :: "i"(WIN - 1) : "memory")

__device__ __forceinline__ float asf(u32 x) { return __builtin_bit_cast(float, x); }
__device__ __forceinline__ u32 asu(float x) { return __builtin_bit_cast(u32, x); }
__device__ __forceinline__ float lo16(u32 x) { return asf(x << 16); }
__device__ __forceinline__ float hi16(u32 x) { return asf(x & 0xffff0000u); }
__device__ __forceinline__ u32 pk(float a, float b) { v2bf v = {(__bf16)a, (__bf16)b}; return __builtin_bit_cast(u32, v); }
__device__ __forceinline__ float rb(float a) { return (float)(__bf16)a; }
__device__ __forceinline__ void rb2(float& a, float& b) { const u32 t = pk(a, b); a = lo16(t); b = hi16(t); }
template <int C> __device__ __forceinline__ float dpp(float x) { return asf((u32)__builtin_amdgcn_update_dpp(0, (int)asu(x), C, 0xf, 0xf, true)); }
template <int C> __device__ __forceinline__ u32 dppu(u32 x) { return (u32)__builtin_amdgcn_update_dpp(0, (int)x, C, 0xf, 0xf, true); }
constexpr int QX1 = 0xB1, QX2 = 0x4E, HMIR = 0x141, ROR1 = 0x121, ROR2 = 0x122, ROR4 = 0x124, ROR8 = 0x128;
__device__ __forceinline__ float t32_add(float a, float b) { auto s = __builtin_amdgcn_permlane32_swap(asu(a), asu(b), false, false); return asf(s[0]) + asf(s[1]); }
__device__ __forceinline__ float t16_add(float a, float b) { auto s = __builtin_amdgcn_permlane16_swap(asu(a), asu(b), false, false); return asf(s[0]) + asf(s[1]); }
__device__ __forceinline__ float x16_max(float x) { auto s = __builtin_amdgcn_permlane16_swap(asu(x), asu(x), false, false); return fmaxf(asf(s[0]), asf(s[1])); }
__device__ __forceinline__ float x32_max(float x) { auto s = __builtin_amdgcn_permlane32_swap(asu(x), asu(x), false, false); return fmaxf(asf(s[0]), asf(s[1])); }
__device__ __forceinline__ float rows4(float a0, float a1, float a2, float a3) { return t16_add(t32_add(a0, a2), t32_add(a1, a3)); }
__device__ __forceinline__ float sum_lo3(float x) { x += dpp<QX1>(x); x += dpp<QX2>(x); x += dpp<HMIR>(x); return x; }
__device__ __forceinline__ float max_lo3(float x) { x = fmaxf(x, dpp<QX1>(x)); x = fmaxf(x, dpp<QX2>(x)); x = fmaxf(x, dpp<HMIR>(x)); return x; }
__device__ __forceinline__ float sum_row(float x) { x = sum_lo3(x); return x + dpp<ROR8>(x); }
__device__ __forceinline__ float max_row(float x) { x = max_lo3(x); return fmaxf(x, dpp<ROR8>(x)); }
__device__ __forceinline__ v4f mfma(v4u a, v4u b, v4f c) {
    return __builtin_amdgcn_mfma_f32_16x16x32_bf16(__builtin_bit_cast(v8bf, a), __builtin_bit_cast(v8bf, b), c, 0, 0, 0);
}
__device__ __forceinline__ v4f mfma16(v2u a, v2u b, v4f c) {
    return __builtin_amdgcn_mfma_f32_16x16x16bf16_1k(__builtin_bit_cast(v4s, a), __builtin_bit_cast(v4s, b), c, 0, 0, 0);
}
__device__ __forceinline__ bool all_ok(bool ok) { return __builtin_amdgcn_ballot_w64(!ok) == 0ull; }
__device__ __forceinline__ float silu(float v) { return v * __builtin_amdgcn_rcpf(1.f + __builtin_amdgcn_exp2f(v * -1.44269504089f)); }
__device__ __forceinline__ float rdl(float v, int lane) { return asf((u32)__builtin_amdgcn_readlane((int)asu(v), lane)); }
constexpr float SC = 0.0625f * 1.44269504089f;

__device__ __forceinline__ void tr8(const v4u* r, v4u* o) {
#pragma unroll
    for (int j = 0; j < 8; ++j) {
        const u32 sel = (j & 1) ? 0x07060302u : 0x05040100u;
#pragma unroll
        for (int t = 0; t < 4; ++t) o[j][t] = __builtin_amdgcn_perm(r[2 * t + 1][j >> 1], r[2 * t][j >> 1], sel);
    }
}
__device__ __forceinline__ v4u seal(v4f a, u32 tag) {
    return v4u{__builtin_amdgcn_perm(asu(a[0]), tag, 0x07060500u), __builtin_amdgcn_perm(asu(a[1]), tag, 0x07060501u),
               __builtin_amdgcn_perm(asu(a[2]), tag, 0x07060502u), __builtin_amdgcn_perm(asu(a[3]), tag, 0x07060503u)};
}
__device__ __forceinline__ v4u tag_bytes(u32 t) { return v4u{t & 255u, (t >> 8) & 255u, (t >> 16) & 255u, t >> 24}; }
__device__ __forceinline__ v4u poll16(rsrc_t rs, u32 off, v4u tb) {
    v4u y;
    SPIN(SPIN_CAP) {
        y = LD_C4(rs, off) ^ tb;
        FENCE();
        if (NONE((y.x | y.y | y.z | y.w) & 255u)) break;
    }
    return y;
}

#define STAMP(i)
#define DUMP()

__device__ __forceinline__ void ln_part(char* lds, int w, int l, float a0, float a1) {
    const float K0 = asf((u32)__builtin_amdgcn_readfirstlane((int)asu(a0))), K1 = asf((u32)__builtin_amdgcn_readfirstlane((int)asu(a1)));
    const float d0 = a0 - K0, d1 = a1 - K1;
    float u = sum_row(rows4(d0, d1, d0 * d0, d1 * d1));
    const bool isk = (l & 47) == 1;
    u = isk ? ((l & 16) ? K1 : K0) : u;
    if ((l & 15) == 0 || isk) *(float*)(lds + L_ST + ((l >> 4) & 1) * 128 + w * 16 + (isk ? 8 : (l >> 5) * 4)) = u;
}
__device__ __forceinline__ void ln_fin(char* lds, int l, float& a0, float& a1) {
    const v4f s = *(const v4f*)(lds + L_ST + (l & 15) * 16);
    const bool live = (l & 7) != 7;
    const float M = sum_lo3(live ? s.x + 64.f * s.z : 0.f) * (1.f / 448);
    const float e = M - s.z;
    const float V = sum_lo3(live ? s.y + e * (64.f * e - 2.f * s.x) : 0.f) * (1.f / 448);
    const float r = __builtin_amdgcn_rcpf(rb(__builtin_amdgcn_sqrtf(rb(rb(V) + 1e-5f)))), Mb = rb(M);
    a0 -= rdl(Mb, 0); a1 -= rdl(Mb, 8);
    rb2(a0, a1);
    a0 *= rdl(r, 0); a1 *= rdl(r, 8);
    rb2(a0, a1);
}

template <int QD, int JJ> __device__ __forceinline__ u32 bank_mov(u32 old, u32 P) {
    constexpr int C = QD == JJ ? 0xE4 : QD > JJ ? 0x110 + 4 * (QD - JJ) : 0x100 + 4 * (JJ - QD);
    return (u32)__builtin_amdgcn_update_dpp((int)old, (int)P, C, 0xf, 1 << QD, false);
}
template <int QD> __device__ __forceinline__ void bank4(v4u& A, u32 P) {
    A[0] = bank_mov<QD, 0>(A[0], P); A[1] = bank_mov<QD, 1>(A[1], P); A[2] = bank_mov<QD, 2>(A[2], P); A[3] = bank_mov<QD, 3>(A[3], P);
}
__device__ __forceinline__ void build_a(u32 hv, u32 psel, v4u* A) {
    const u32 P = __builtin_amdgcn_perm(dppu<QX1>(hv), hv, psel);
    bank4<0>(A[0], P); bank4<1>(A[1], P); bank4<2>(A[2], P); bank4<3>(A[3], P);
}
__device__ __forceinline__ void zero_a(v4u* A) {
#pragma unroll
    for (int i = 0; i < 4; ++i) { A[i] = v4u{0, 0, 0, 0}; asm volatile("" : "+v"(A[i])); }
}
__device__ __forceinline__ v4f gemv(const v4u* A, const v4u* B) {
    v4f a0 = {0, 0, 0, 0}, a1 = {0, 0, 0, 0};
#pragma unroll
    for (int qd = 0; qd < 4; ++qd) {
        a0 = mfma(A[qd], B[2 * qd], a0);
        a1 = mfma(A[qd], B[2 * qd + 1], a1);
    }
    return v4f{a0[0] + dpp<ROR8>(a0[2]), a0[1] + dpp<ROR8>(a0[3]), a1[0] + dpp<ROR8>(a1[2]), a1[1] + dpp<ROR8>(a1[3])};
}
__device__ __forceinline__ u32 wvo(int l, u32 pitch) { return ((l >> 4) * 16 + ((l >> 3) & 1) * 2) * pitch + (l & 7) * 16; }
__device__ __forceinline__ v2f sum7(const char* p) {
    v2f s = *(const v2f*)p;
#pragma unroll
    for (int i = 1; i < 7; ++i) s += *(const v2f*)(p + i * 512);
    return s;
}

__global__ void __launch_bounds__(512, 2)
adaln_block_kernel(const char*, const char*, const char*, const char*, const char*, char*, const char*, const char*,
                   const char*, const char*, char*, char*) {
    __shared__ __attribute__((aligned(16))) char lds[L_END];
    STAMP(0);
    const u64 pt0 = __builtin_amdgcn_s_memrealtime();
    u64 pt1 = 0;
    v16u ka;
    v8u kb;
    u32 bid = blockIdx.x, tid = threadIdx.x;
    asm volatile("s_load_dwordx16 %0, %3, 0x0\n\ts_load_dwordx8 %1, %3, 0x40"
                 : "=&s"(ka), "=&s"(kb), "+s"(bid) : "s"(__builtin_amdgcn_kernarg_segment_ptr()));
    asm volatile("" : "+v"(tid));
    u32 pfv = 0;
    {
        u64 pc; u32 len;
        asm volatile("s_getpc_b64 %0\n.Ladaln_pc:\n\ts_max_i32 %1, .Ladaln_e0-.Ladaln_pc, 0\n\ts_max_i32 %1, .Ladaln_e1-.Ladaln_pc, %1\n\t"
                     "s_max_i32 %1, .Ladaln_e2-.Ladaln_pc, %1" : "=s"(pc), "=s"(len) :: "scc");
        const rsrc_t rsc = RS(pc, bid < PFWG * 8 ? len : 0);
        pfv = __builtin_amdgcn_raw_buffer_load_b32(rsc, tid * 128, 0, 0);
        asm volatile("" : "+s"(bid) :: "memory");
    }
    const u32 late4 = bid & 4, idx = bid >> 3, xc = bid & 7;
    const int rel = idx + late4 - 4;
    const bool isX = rel < 0, isQ = rel < 8;
    u32 ng = (u32)((((u64)LSZ << 32) | ESZ) >> (xc * 8)) & 255u;
    u32 g0 = ((idx >> 2) + (late4 >> 1) - 3) * ng;
    g0 = late4 ? 256 - g0 : g0;
    u32 slab = ((bid >> 1) & 12) | (bid & 3);
    u32 qn = xc * 8 + idx;
    KEEPS(qn);
    qn += late4 - 4;
    u32 split = (late4 ? 3 : -2) + (rel >> 2);
    {
        const u32 sh = (xc & 3) * 8, e5 = ((ESZ >> sh) & 255) * 5, qe = (QSZ >> sh) & 255, rem = 256 - e5 - ((LSZ >> sh) & 255) * 6 - 2 * qe, hi = (rel >> 2) & 1;
        const u32 qg = e5 + (late4 ? 2 * qe + (hi ? rem >> 1 : 0) : hi * qe), qk = late4 ? (hi ? rem - (rem >> 1) : rem >> 1) : qe;
        g0 = isQ ? qg : g0;
        ng = isQ ? qk : ng;
        split = isQ ? 11 + (late4 >> 1) + hi : split;
    }
    u32 kvb = ((slab << 10) + 0x300 + g0) << 12, kvn = ng << 12;
    int fu = (rel - 8) * 8 + xc;
    const bool isF = !isQ && fu < 28;
    const int w = __builtin_amdgcn_readfirstlane(tid >> 6);
    const int l = tid & 63, col = tid;
    u32 psel = (l & 1) ? 0x03020706u : 0x05040100u;
    const int gpos = ((l & 7) * 8 + (l >> 4) * 2) * 8;
    u32 kvo = (tid & 7) * 512 + (tid & 8) * 8 + (tid & 48);
    u32 oa = WS_O + ((slab * 2 + (w & 1)) * 16 + split) * 1024 + l * 16;
    u32 ra = L_OP + (w & 1) * 1024 + l * 8;
    const u32 pitch = isX ? 5376u : 8192u;
    u32 n0 = isQ ? pitch * 448u : kvn, nx = isQ ? 1792u : 0u, ko = isQ ? (isX ? slab : qn) << 7 : kvb;
    u32 s1 = isQ ? pitch : 128u, s2 = isQ ? pitch * 4 : 256u, s3 = s1 + s2;
    u32 vo = isQ ? ((tid & 0x1f0) | ((tid >> 2) & 2)) * pitch + (tid & 7) * 16 : (tid >> 6) * 4096 + kvo;
    u32 vo2 = vo + (isQ ? pitch * 8 : 28672u), xo2 = tid * 2;
    asm volatile("" : "+v"(vo), "+v"(vo2), "+v"(xo2), "+v"(kvo), "+v"(oa), "+v"(ra), "+v"(psel));
    asm volatile("" : "+s"(kvb), "+s"(kvn), "+s"(ng), "+s"(split), "+s"(slab), "+s"(qn), "+s"(fu));
    STAMP(3);
    asm volatile("s_waitcnt lgkmcnt(0)" : "+s"(ka), "+s"(kb), "+s"(n0), "+s"(nx), "+s"(ko), "+s"(s1), "+s"(s2), "+s"(s3));
    STAMP(4);
    u32 tag = *(const __attribute__((address_space(4))) u32*)(u64)ws;
#define GET_TAG() do { KEEPS(tag); tag = tag * 1664525u + 1013904223u; } while (0)
    const rsrc_t rsW = RS(ws, WS_END);
    v4u G[8];

    if (__builtin_expect(w < 7, 1)) {
        v4u R[8];
        u32 xi0, xi1;
        {
            const rsrc_t rsv = RS(isX ? cin : xin, nx);
            xi0 = __builtin_amdgcn_raw_buffer_load_b16(rsv, xo2, 0, 0);
            xi1 = __builtin_amdgcn_raw_buffer_load_b16(rsv, xo2 + 896, 0, 0);
            const char* fp = isX ? wmod : wq;
            const rsrc_t rs0 = RS((isQ ? fp : kc) + ko, n0);
#define LD0(i) R[i] = LD_NT(rs0, (i) < 4 ? vo : vo2, ((i) & 3) == 0 ? 0u : ((i) & 3) == 1 ? s1 : ((i) & 3) == 2 ? s2 : s3)
#pragma unroll
            for (int i = 0; i < W0; ++i) LD0(i);
            FENCE();
            if (!isQ) asm volatile("s_waitcnt vmcnt(%0)" :: "i"(W0 - 1) : "memory");
#pragma unroll
            for (int i = W0; i < W0 + W1; ++i) LD0(i);
            FENCE();
            if (!isQ) asm volatile("s_waitcnt vmcnt(%0)" :: "i"(W1) : "memory");
#pragma unroll
            for (int i = W0 + W1; i < 8; ++i) LD0(i);
            FENCE();
            PIN();
        }
        if (isX) {
            if (slab >= 14) EXIT();
            const rsrc_t rs0 = RS(wmod, 448 * 5376);
            u32 so = slab * 128;
            v4u B[8], A[4];
            zero_a(A);
            build_a(pk(silu(lo16(xi0)), silu(lo16(xi1))), psel, A);
#pragma unroll 1
            for (int u = 0; u < 3; ++u) {
                tr8(R, B);
                KEEP8(B);
                if (u == 0) STAMP(2);
                const v4f t = gemv(A, B);
                if (!(l & 8)) *(v4f*)(lds + L_P1 + (u & 1) * 3584 + w * 512 + gpos) = t;
                BAR();
                so = u == 2 ? OOB : so + 14 * 128;
#pragma unroll
                for (int i = 0; i < 8; ++i) R[i] = LD_NT(rs0, vo, so + ((i >> 1) * 4 + (i & 1)) * 5376);
                FENCE();
            }
            asm volatile(".Ladaln_e2:");
            DUMP();
            EXIT();
        }
        GET_TAG();
        v4u K2[4], V[12];
        const rsrc_t rsK = RS(kc + kvb, kvn), rsV = RS(vc + kvb, kvn);
        u32 sl[3] = {w * 4096u, w * 4096u + 28672u, w * 4096u + 57344u};
        asm volatile("" : "+s"(sl[0]), "+s"(sl[1]), "+s"(sl[2]));
#define LDK(i) LD_NT(rsK, kvo + ((i) & 3) * 128, sl[(i) >> 2])
#define LDV(i) V[i] = LD_NT(rsV, l * 16 + ((i) & 3) * 1024, sl[(i) >> 2])
#define LDG(i) G[i] = LD_NT(rsG, ((i) & 1) ? gv1 : gv0, gs[(i) >> 1])
        char* pw = lds + L_PW + w * 256;
        if (l < 32) *(u32*)(pw + 128 + l * 4) = 0;
        float a0 = lo16(xi0), a1 = lo16(xi1);
        if (isQ) ln_part(lds, w, l, a0, a1);
        BAR();
        if (isQ) {
            ln_fin(lds, l, a0, a1);
            KEEP(a0); KEEP(a1);
            v4u B[8], A[4];
            zero_a(A);
            tr8(R, B);
            KEEP8(B);
            PIN();
            STAMP(1);
            v2u sa, ca;
            SPIN(SPIN_CAP) {
                sa = LD_C2(rsW, WS_MOD + col * 8);
                ca = LD_C2(rsW, WS_MOD + 3584 + col * 8);
                FENCE();
                if (NONE((sa.y ^ tag) | (ca.y ^ tag))) break;
            }
            STAMP(2);
            build_a(pk(rb(a0 * rb(1.f + lo16(ca.x))) + lo16(sa.x), rb(a1 * rb(1.f + hi16(ca.x))) + hi16(sa.x)), psel, A);
            const v4f t = gemv(A, B);
            if (!(l & 8)) *(v4f*)(lds + L_P1 + w * 512 + gpos) = t;
            BAR();
#pragma unroll
            for (int i = 0; i < 8; ++i) R[i] = LDK(i);
            FENCE(); PIN();
        }
#pragma unroll
        for (int i = 0; i < 4; ++i) { WINWAIT(); K2[i] = LDK(8 + i); }
        const int row = l & 15, kq = l >> 4;
        const rsrc_t rsG = RS(isQ ? wo + (size_t)((qn >> 2) * 256 + (qn & 3) * 2) * 896 : w2 + (size_t)(isF ? fu : 0) * 57344, isQ ? 250 * 896 : isF ? 57344 : 0);
        u32 gv0 = (isQ ? kq * 32 + (row >> 3) : kq * 16 + (row >> 3) * 2) * 896 + (l & 7) * 16, gv1 = gv0 + (isQ ? 128 * 896 : 896);
        const u32 gst = isQ ? 7168u : 3584u;
        u32 gs[4] = {w * 128u, w * 128u + gst, w * 128u + 2 * gst, w * 128u + 3 * gst};
        u32 wa = L_OP + w * 2048 + l * 8;
        asm volatile("" : "+v"(gv0), "+v"(gv1), "+v"(wa));
        asm volatile("" : "+s"(gs[0]), "+s"(gs[1]), "+s"(gs[2]), "+s"(gs[3]));
        float s = 0;
        v4u PA[4];
        v2u PB[4];
#pragma unroll
        for (int i = 0; i < 12; ++i) {
            WINWAIT(); LDV(i);
            FENCE(); PIN();
            if (i == QPOS) {
                for (;;) {
                    FENCE();
                    if (__builtin_amdgcn_readfirstlane(*(const u32*)(lds + L_FLG))) break;
                    __builtin_amdgcn_s_sleep(1);
                }
                FENCE();
                STAMP(6);
                const char* qb = lds + L_QS + (row & 1) * 512 + ((row >> 1) & 1) * 64 + kq * 16;
                v4f sc[3];
#pragma unroll
                for (int g = 0; g < 3; ++g) sc[g] = v4f{0, 0, 0, 0};
#pragma unroll
                for (int j = 0; j < 4; ++j) {
                    const v4u A = *(const v4u*)(qb + j * 128);
#pragma unroll
                    for (int g = 0; g < 3; ++g) sc[g] = mfma(A, g < 2 ? R[g * 4 + j] : K2[j], sc[g]);
                }
                const v4f sg = kq == 0 ? sc[0] : kq == 1 ? sc[1] : sc[2];
                s = (row < 8 ? sg[0] : sg[3]) + dpp<ROR8>(row < 8 ? sg[1] : sg[2]);
                s = (kq < 3 && kq * 7 + w < (int)ng) ? rb(s) : -INFINITY;
                PIN();
            }
            if (i == QPOS + 1) {
                const float m = x32_max(x16_max(max_lo3(s)));
                const float p = __builtin_amdgcn_exp2f((s - (m == -INFINITY ? 0.f : m)) * SC);
                *(u16*)(pw + (kq >> 1) * 64 + (l & 1) * 32 + ((l >> 3) & 1) * 16 + ((kq & 1) * 4 + ((l >> 1) & 3)) * 2) = (u16)pk(p, p);
                float ls = sum_lo3(p);
                ls = t16_add(ls, ls);
                ls = t32_add(ls, ls);
                if ((l & 55) == 0) *(v2f*)(lds + L_ML + (l + w) * 8) = v2f{m, ls};
                PIN();
            }
            if (i == QPOS + 2) {
                LDSWAIT();
                const int real = (kq >> 1) * 32 + ((row >> 1) & 1) * 16;
                const bool on = (kq & 1) == (row & 1);
#pragma unroll
                for (int qd = 0; qd < 4; ++qd) {
                    const char* ap = pw + ((on && (row >> 2) == qd) ? real : 128);
                    PA[qd] = *(const v4u*)ap;
                    PB[qd] = *(const v2u*)(ap + 64);
                }
                PIN();
            }
        }
        STAMP(5);
        WINWAIT(); LDG(0);
        FENCE(); PIN();
        v4f o0 = {0, 0, 0, 0}, o1 = {0, 0, 0, 0};
        {
            v4u BV[8];
            tr8(V, BV);
#pragma unroll
            for (int qd = 0; qd < 4; ++qd) {
                o0 = mfma(PA[qd], BV[2 * qd], o0);
                o1 = mfma(PA[qd], BV[2 * qd + 1], o1);
            }
        }
        KEEP(o0); KEEP(o1);
        PIN();
        STAMP(7);
#pragma unroll
        for (int i = 1; i < 3; ++i) { WINWAIT(); LDG(i); }
        FENCE(); PIN();
        {
            v2u BV[8];
#pragma unroll
            for (int j = 0; j < 8; ++j) BV[j].x = __builtin_amdgcn_perm(V[9][j >> 1], V[8][j >> 1], (j & 1) ? 0x07060302u : 0x05040100u);
            PIN();
            WINWAIT();
#pragma unroll
            for (int i = 3; i < 8; ++i) LDG(i);
            FENCE(); PIN();
#pragma unroll
            for (int j = 0; j < 8; ++j) BV[j].y = __builtin_amdgcn_perm(V[11][j >> 1], V[10][j >> 1], (j & 1) ? 0x07060302u : 0x05040100u);
#pragma unroll
            for (int qd = 0; qd < 4; ++qd) {
                o0 = mfma16(PB[qd], BV[2 * qd], o0);
                o1 = mfma16(PB[qd], BV[2 * qd + 1], o1);
            }
        }
        *(v2f*)(lds + wa) = v2f{o0[0], o0[1]};
        *(v2f*)(lds + wa + 1024) = v2f{o0[2], o0[3]};
        *(v2f*)(lds + wa + 512) = v2f{o1[0], o1[1]};
        *(v2f*)(lds + wa + 1536) = v2f{o1[2], o1[3]};
        FENCE();
    } else {
        GET_TAG();
        __builtin_amdgcn_raw_buffer_store_b32((u32)pt0, rsW, l == 0 ? 1024 + blockIdx.x * 4 : OOB, 0, 16);
        if (isX) {
            if (slab >= 14) EXIT();
#pragma unroll 1
            for (int u = DRY ? -1 : 0; u < 3; ++u) {
                if (u >= 0) BAR();
                const v2f s = sum7(lds + L_P1 + (u & 1) * 3584 + l * 8);
                ST_C2((v2u{pk(s.x, s.y), tag}), rsW, u < 0 ? OOB : WS_MOD + ((slab + u * 14) * 64 + l) * 8);
                if (u == 0) STAMP(3); else STAMP(4);
            }
            DUMP();
            EXIT();
        }
        if (l < 16) *(v2u*)(lds + L_ML + l * 8) = (l & 7) == 7 ? v2u{0xff800000u, 0u} : v2u{~0u, ~0u};
        if (l == 0) *(u32*)(lds + L_FLG) = 0;
        BAR();
        if (isQ) {
            BAR();
            const v2f s = sum7(lds + L_P1 + l * 8);
            const u32 hv = pk(s.x, s.y);
            ST_C2((v2u{__builtin_amdgcn_perm(dppu<QX1>(hv), hv, psel), tag}), rsW, WS_Q + (l & 1) * 16384 + (qn * 32 + (l >> 1)) * 8);
            STAMP(3);
        } else __builtin_amdgcn_s_sleep(QSLEEP);
        v4u qa, qb;
        const u32 qo = WS_Q + slab * 2048 + l * 16;
        SPIN(SPIN_CAP) {
            qa = LD_C4(rsW, qo);
            qb = LD_C4(rsW, qo + 1024);
            FENCE();
            if (NONE((qa.y ^ tag) | (qa.w ^ tag) | (qb.y ^ tag) | (qb.w ^ tag))) break;
        }
        *(v2u*)(lds + L_QS + l * 8) = v2u{qa.x, qa.z};
        *(v2u*)(lds + L_QS + 512 + l * 8) = v2u{qb.x, qb.z};
        LDSWAIT();
        *(u32*)(lds + L_FLG) = 1;
        pt1 = __builtin_amdgcn_s_memrealtime();
        FENCE();
        STAMP(5);
        v2u ml;
        SPIN(SPIN_CAP * 16) {
            FENCE();
            ml = *(const v2u*)(lds + L_ML + (l & 15) * 8);
            if (all_ok(ml.x != ~0u && ml.y != ~0u)) break;
            __builtin_amdgcn_s_sleep(1);
        }
        const float M = max_lo3(asf(ml.x));
        const float f = __builtin_amdgcn_exp2f((asf(ml.x) - M) * SC);
        const float L = sum_lo3(f * asf(ml.y));
        if (l < 16) *(float*)(lds + L_F + l * 4) = f;
        if ((l & 55) == 0) ST_C4((v4u{asu(M), tag, asu(L), tag}), rsW, WS_ST + ((slab * 2 + (l >> 3)) * 16 + split) * 16);
        STAMP(7);
        asm volatile(".Ladaln_e1:");
        ANY8(G);
    }
#pragma unroll 1
    for (int it = (DRY && w == 7) ? 0 : 1; it < 2; ++it) {
        if (it) BAR();
        if (w >= 6) {
            const char* fp = lds + L_F + (w & 1) * 32;
            v4f fa = *(const v4f*)fp, fb = *(const v4f*)(fp + 16);
            v2f x[7], y[7];
#pragma unroll
            for (int i = 0; i < 7; ++i) { x[i] = *(const v2f*)(lds + ra + i * 2048); y[i] = *(const v2f*)(lds + ra + i * 2048 + 512); }
            PIN();
            v2f ax = x[0] * fa[0], ay = y[0] * fa[0];
#pragma unroll
            for (int i = 1; i < 7; ++i) { const float f = i < 4 ? fa[i] : fb[i - 4]; ax += x[i] * f; ay += y[i] * f; }
            ST_C4(seal(v4f{ax.x, ax.y, ay.x, ay.y}, tag), rsW, it ? oa : OOB);
        }
    }
    STAMP(9);
    if (!isQ && !isF) { DUMP(); EXIT(); }

    v4u BG[8], B[8], A[4], R[8];
    zero_a(A);
    ANY8(BG); ANY8(B); ANY8(R);
    u32 rel4 = idx + late4;
    KEEPS(rel4);
    const bool onlyQ = rel4 < 12;
    const int hq = qn >> 2, sp4 = (w >> 1) * 4 + (l >> 4), bb = (l >> 3) & 1, c7 = fu * 16 + ((l & 31) >> 1);
    const u32 po = ((l >> 3) * 8 + w) * 3584 + fu * 128 + (l & 7) * 16;
    const u32 oo = sp4 == 15 ? OOB : WS_O + ((bb * 16 + hq) * 16 + sp4) * 1024 + (qn & 3) * 256 + (w & 1) * 128 + (l & 7) * 16;
    u32 off = isQ ? oo : WS_WP + po;
    v4u tb = tag_bytes((isQ && sp4 == 15) ? 0u : tag);
    char* wa = lds + (isQ ? L_P1 + (w >> 1) * 512 + ((w & 1) * 32 + (l & 7) * 4 + (l >> 4)) * 8 + bb * 4 : L_RR + ((l & 7) * 4 + (l >> 4)) * 32 + w * 4);
    const u32 go = (l & 8) ? OOB : (isQ ? WS_WP + qn * 3584 : WS_FP + fu * 3584) + w * 512 + gpos;
    const u32 mo0 = WS_MOD + (w < 7 ? 1344 + col : 896 + c7) * 8, mo1 = WS_MOD + (w < 7 ? 1792 + col : 2240 + c7) * 8;
    const u32 xo = (l & 1) * 896 + c7 * 2;
    const rsrc_t rso = RS(out, 1792);
    v2u ma = {0, 0}, mb = {0, 0};
    u32 xv = 0;
    if (isF) {
        ma = LD_C2(rsW, mo0);
        mb = LD_C2(rsW, mo1);
        xv = __builtin_amdgcn_raw_buffer_load_b16(RS(xin, 1792), xo, 0, 0);
        const rsrc_t rs1 = RS(w1, 448 * 3584);
        const u32 v1 = wvo(l, 3584);
#pragma unroll
        for (int i = 0; i < 8; ++i) R[i] = LD_NT(rs1, v1, w * 64 * 3584 + fu * 128 + ((i >> 1) * 4 + (i & 1)) * 3584);
        FENCE();
    }
    if (onlyQ && w < 3) *(v2f*)(lds + L_P1 + (4 + w) * 512 + l * 8) = v2f{0.f, 0.f};
    float fac = 1.f;
    float m0 = 0, m1 = 0, m2 = 0, m3 = 0;
#pragma unroll 1
    for (int ph = DRY ? 0 : 1; ph < 3; ++ph) {
        v4u y = {0, 0, 0, 0};
        if (ph == 1) {
            if (onlyQ) {
                const bool dead = (l & 15) == 15;
                const u32 tl = dead ? 0u : tag;
                v4u st;
                SPIN(SPIN_CAP) {
                    st = LD_C4(rsW, dead ? OOB : WS_ST + (((l & 16) + hq) * 16 + (l & 15)) * 16);
                    FENCE();
                    if (NONE((st.y ^ tl) | (st.w ^ tl))) break;
                }
                const float m = dead ? -INFINITY : asf(st.x);
                float f = __builtin_amdgcn_exp2f((m - max_row(m)) * SC);
                f *= __builtin_amdgcn_rcpf(sum_row(f * asf(st.z)));
                fac = asf((u32)__builtin_amdgcn_ds_bpermute((bb * 16 + sp4) * 4, (int)asu(f)));
            }
            if (isF) {
                SPIN(SPIN_CAP) {
                    if (NONE((ma.y ^ tag) | (mb.y ^ tag))) break;
                    ma = LD_C2(rsW, mo0);
                    mb = LD_C2(rsW, mo1);
                    FENCE();
                }
                asm volatile("" : "+v"(xv));
                const float ga = (l & 1) ? hi16(ma.x) : lo16(ma.x), gb = (l & 1) ? hi16(mb.x) : lo16(mb.x), c1 = rb(1.f + hi16(mb.x));
                m0 = lo16(w < 7 ? ma.x : xv); m1 = w < 7 ? hi16(ma.x) : ga; m2 = rb(1.f + lo16(mb.x)); m3 = w < 7 ? c1 : gb;
                KEEP8(R);
                tr8(R, B);
            }
            KEEP8(G);
            tr8(G, BG);
            KEEP(fac);
            PIN();
            STAMP(10);
        }
        if (ph) {
            y = poll16(rsW, off, tb);
            if (ph == 1) STAMP(11); else STAMP(14);
        }
        float t = rows4(asf(y.x) * fac, asf(y.y) * fac, asf(y.z) * fac, asf(y.w) * fac);
        const float t2 = t + dpp<ROR8>(t);
        *(float*)wa = isQ ? t : t2;
        BAR();
        if (w == 7 && isF) {
            const v4f q = *(const v4f*)(lds + L_RR + (l & 31) * 32 + (l >> 5) * 16);
            float s = (q[0] + q[1]) + (q[2] + q[3]);
            s = t32_add(s, s);
            const u32 yb = pk(m0 + rb(m1 * rb(s)), 0.f);
            ST_C2((v2u{yb | (dppu<QX1>(yb) << 16), tag}), rsW, (ph == 1 && !(l & 33)) ? WS_X1 + c7 * 8 : OOB);
            __builtin_amdgcn_raw_buffer_store_b16((u16)yb, rso, (ph == 2 && l < 32) ? xo : OOB, 0, 0);
            __builtin_amdgcn_raw_buffer_store_b32(tag, rsW, (ph == 2 && fu == 0 && l == 0) ? 0 : OOB, 0, 16);
            m0 = lo16(yb); m1 = m3;
            if (ph == 1) STAMP(12);
        }
        if (ph == 2 && fu == 0 && w == 7) {
            const u64 now = __builtin_amdgcn_s_memrealtime();
            const v4u sv = LD_C4(rsW, 1024 + l * 16);
            int a = (int)(sv.x - (u32)pt0), b = (int)(sv.y - (u32)pt0), c = (int)(sv.z - (u32)pt0), d = (int)(sv.w - (u32)pt0);
            int mx = max(max(a, b), max(c, d)), mn = min(min(a, b), min(c, d)), sm = a + b + c + d;
            for (int o = 1; o < 64; o <<= 1) { mx = max(mx, __shfl_xor(mx, o)); mn = min(mn, __shfl_xor(mn, o)); sm += __shfl_xor(sm, o); }
            int q = PROBE == 0 ? 0 : PROBE == 1 ? (int)(now - pt0) : PROBE == 2 ? mx : PROBE == 3 ? -mn : PROBE == 4 ? (sm >> 8) - mn : (int)(pt1 - pt0);
            q = __builtin_amdgcn_readfirstlane(q);
            q = q < 0 ? 0 : q > 5000 ? 5000 : q;
            const u64 tgt = pt0 + 6000 + 4 * q;
            while (__builtin_amdgcn_s_memrealtime() < tgt) __builtin_amdgcn_s_sleep(1);
        }
        if (ph == 2) { STAMP(15); DUMP(); EXIT(); }
        if (isF) {
            float a0, a1;
            if (w < 7) {
                v2u xg = {0, 0};
                if (ph) {
                    SPIN(SPIN_CAP) {
                        xg = LD_C2(rsW, WS_X1 + col * 8);
                        FENCE();
                        if (NONE(xg.y ^ tag)) break;
                    }
                    STAMP(12);
                }
                a0 = lo16(xg.x); a1 = hi16(xg.x);
                ln_part(lds, w, l, a0, a1);
            }
            BAR();
            if (w < 7) {
                ln_fin(lds, l, a0, a1);
                a0 *= m2; a1 *= m3;
                rb2(a0, a1);
                build_a(pk(a0 + m0, a1 + m1), psel, A);
                const v4f t1 = gemv(A, B);
                if (!(l & 8)) *(v4f*)(lds + L_P1 + w * 512 + gpos) = t1;
            }
            BAR();
        }
        if (w < 7) {
            const v2f s = sum7(lds + L_P1 + l * 8);
            u32 hv = pk(s.x, s.y);
            if (isF) hv = pk(silu(lo16(hv)), silu(hi16(hv)));
            build_a(hv, psel, A);
            ST_C4(seal(gemv(A, BG), tag), rsW, ph ? go : OOB);
            if (ph) STAMP(13);
        }
        if (ph) {
            if (isQ) { DUMP(); EXIT(); }
            const bool dead = (l >> 3) * 8 + w >= 28;
            off = dead ? OOB : WS_FP + po;
            tb = tag_bytes(dead ? 0u : tag);
        }
    }
    asm volatile(".Ladaln_e0:" :: "v"(pfv));
}

extern "C" void kernel_launch(void* const* d_in, const int* in_sizes, int n_in, void* d_out, int out_size, void* d_ws, size_t ws_size,
                              hipStream_t stream) {
    adaln_block_kernel<<<256, 512, 0, stream>>>((const char*)d_in[1], (const char*)d_in[3], (const char*)d_in[9], (const char*)d_in[8],
                                                (const char*)d_in[0], (char*)d_ws, (const char*)d_in[2], (const char*)d_in[4],
                                                (const char*)d_in[6], (const char*)d_in[7], (char*)d_out, (char*)d_out);
}
```

```cpp
#include <hip/hip_runtime.h>

typedef unsigned u32;
typedef unsigned long long u64;
typedef unsigned short u16;
typedef u32 v4u __attribute__((ext_vector_type(4)));
typedef u32 v3u __attribute__((ext_vector_type(3)));
typedef u32 v2u __attribute__((ext_vector_type(2)));
typedef float v4f __attribute__((ext_vector_type(4)));
typedef float v2f __attribute__((ext_vector_type(2)));
typedef u32 v8u __attribute__((ext_vector_type(8)));
typedef u32 v16u __attribute__((ext_vector_type(16)));
typedef __bf16 v8bf __attribute__((ext_vector_type(8)));
typedef __bf16 v2bf __attribute__((ext_vector_type(2)));
typedef __amdgpu_buffer_rsrc_t rsrc_t;

#define WA 3
#define WB 3
#define WSW 10
#define QD 8
#define SD 24
#define TM 230
#define TP 450
#define PFI 7
#define STR_(x) #x
#define STR(x) STR_(x)

constexpr u32 WS_MP = 2097152;
constexpr u32 MPB = 43008;
constexpr u32 WS_ST = 98304;
constexpr u32 WS_X1 = 106496;
constexpr u32 WS_XS = 114688;
constexpr u32 WS_QP = 1310720;
constexpr u32 WS_WP = 262144;
constexpr u32 WS_FP = 491520;
constexpr u32 WS_O = 720896;
constexpr u32 WS_END = WS_MP + 2 * MPB;
constexpr u32 OOB = 0x40000000u;
#define SPIN_CAP_V 400000u
constexpr u32 SPIN_CAP = SPIN_CAP_V;

constexpr int L_ML = 0;
constexpr int L_QS = 128;
constexpr int L_OP = L_QS + 1280;
constexpr int L_P1 = L_OP + 16384;
constexpr int L_END = L_P1 + 7 * 2064;

#define RS(p, n) __builtin_amdgcn_make_buffer_rsrc((void*)(p), (short)0, (int)(n), 0x00020000)
#define LD_NT(rs, vo, so) __builtin_amdgcn_raw_buffer_load_b128(rs, (int)(vo), (int)(so), 2)
#define LD_C4(rs, vo, so) __builtin_amdgcn_raw_buffer_load_b128(rs, (int)(vo), (int)(so), 16)
#define ST_C4(v, rs, vo) __builtin_amdgcn_raw_buffer_store_b128(v, rs, (int)(vo), 0, 16)
#define ST_C2(v, rs, vo) __builtin_amdgcn_raw_buffer_store_b64(v, rs, (int)(vo), 0, 16)
#define FENCE() asm volatile("" ::: "memory")
#define ARG(v, i) ((char*)(((u64)v[2 * (i) + 1] << 32) | v[2 * (i)]))
#define wo ARG(kb, 0)
#define w1 ARG(kb, 1)
#define w2 ARG(kb, 2)
#define out ARG(kb, 3)
#define EXIT() __builtin_amdgcn_endpgm()
#define KEEP(x) asm volatile("" : "+v"(x))
#define KEEPS(x) asm volatile("" : "+s"(x))
#define KEEP8(x) asm volatile("" : "+v"(x[0]), "+v"(x[1]), "+v"(x[2]), "+v"(x[3]), "+v"(x[4]), "+v"(x[5]), "+v"(x[6]), "+v"(x[7]))
#define ANY8(x) asm volatile("" : "=v"(x[0]), "=v"(x[1]), "=v"(x[2]), "=v"(x[3]), "=v"(x[4]), "=v"(x[5]), "=v"(x[6]), "=v"(x[7]))
__device__ __forceinline__ bool spin_more(u32& n) { asm volatile("" : "+s"(n)); return --n != 0; }
#define SPIN(n) _Pragma("unroll 1") for (u32 sp_ = (n), go_ = 1; go_; go_ = spin_more(sp_))
#define NONE(bad) (__builtin_amdgcn_ballot_w64((bad) != 0u) == 0ull)
#define OFF(c) ((c) ? OOB : 0u)
#define PIN() __builtin_amdgcn_sched_barrier(0)
#define BAR() do { asm volatile("s_waitcnt lgkmcnt(0)\n\ts_barrier" ::: "memory"); } while (0)
#define VMWAIT(n) asm volatile("s_waitcnt vmcnt(%0)" :: "i"(n) : "memory")

__device__ __forceinline__ float asf(u32 x) { return __builtin_bit_cast(float, x); }
__device__ __forceinline__ u32 asu(float x) { return __builtin_bit_cast(u32, x); }
__device__ __forceinline__ v4f asf4(v4u x) { return __builtin_bit_cast(v4f, x); }
__device__ __forceinline__ float lo16(u32 x) { return asf(x << 16); }
__device__ __forceinline__ float hi16(u32 x) { return asf(x & 0xffff0000u); }
__device__ __forceinline__ u32 pk(float a, float b) { v2bf v = {(__bf16)a, (__bf16)b}; return __builtin_bit_cast(u32, v); }
__device__ __forceinline__ float rb(float a) { return (float)(__bf16)a; }
__device__ __forceinline__ void rb2(float& a, float& b) { const u32 t = pk(a, b); a = lo16(t); b = hi16(t); }
template <int C> __device__ __forceinline__ float dpp(float x) { return asf((u32)__builtin_amdgcn_update_dpp(0, (int)asu(x), C, 0xf, 0xf, true)); }
template <int C> __device__ __forceinline__ u32 dppu(u32 x) { return (u32)__builtin_amdgcn_update_dpp(0, (int)x, C, 0xf, 0xf, true); }
constexpr int QX1 = 0xB1, QX2 = 0x4E, HMIR = 0x141, ROR4 = 0x124, ROR8 = 0x128;
__device__ __forceinline__ float t32_add(float a, float b) { auto s = __builtin_amdgcn_permlane32_swap(asu(a), asu(b), false, false); return asf(s[0]) + asf(s[1]); }
__device__ __forceinline__ float t16_add(float a, float b) { auto s = __builtin_amdgcn_permlane16_swap(asu(a), asu(b), false, false); return asf(s[0]) + asf(s[1]); }
__device__ __forceinline__ float fmx(float a, float b) { return __builtin_fmaxf(a, b); }
__device__ __forceinline__ float x32_max(float x) { auto s = __builtin_amdgcn_permlane32_swap(asu(x), asu(x), false, false); return fmx(asf(s[0]), asf(s[1])); }
__device__ __forceinline__ float rows4(v4f a) { return t16_add(t32_add(a[0], a[2]), t32_add(a[1], a[3])); }
__device__ __forceinline__ float sum_lo3(float x) { x += dpp<QX1>(x); x += dpp<QX2>(x); x += dpp<HMIR>(x); return x; }
__device__ __forceinline__ float max_lo3(float x) { x = fmx(x, dpp<QX1>(x)); x = fmx(x, dpp<QX2>(x)); x = fmx(x, dpp<HMIR>(x)); return x; }
__device__ __forceinline__ float sum_row(float x) { x = sum_lo3(x); return x + dpp<ROR8>(x); }
__device__ __forceinline__ float max_row(float x) { x = max_lo3(x); return fmx(x, dpp<ROR8>(x)); }
__device__ __forceinline__ float sum_nb(float x) { x += dpp<QX1>(x); x += dpp<ROR4>(x); x += dpp<ROR8>(x); return t16_add(x, x); }
__device__ __forceinline__ v4f mfma(v4u a, v4u b, v4f c) {
    return __builtin_amdgcn_mfma_f32_16x16x32_bf16(__builtin_bit_cast(v8bf, a), __builtin_bit_cast(v8bf, b), c, 0, 0, 0);
}
__device__ __forceinline__ float silu(float v) { return v * __builtin_amdgcn_rcpf(1.f + __builtin_amdgcn_exp2f(v * -1.44269504089f)); }
__device__ __forceinline__ float rdl(float v, int lane) { return asf((u32)__builtin_amdgcn_readlane((int)asu(v), lane)); }
constexpr float SC = 0.0625f * 1.44269504089f;

template <int T> __device__ __forceinline__ void tr2(const v4u& r0, const v4u& r1, v4u* o) {
#pragma unroll
    for (int j = 0; j < 8; ++j) o[j][T] = __builtin_amdgcn_perm(r1[j >> 1], r0[j >> 1], (j & 1) ? 0x07060302u : 0x05040100u);
}
__device__ __forceinline__ void tr8(const v4u* r, v4u* o) { tr2<0>(r[0], r[1], o); tr2<1>(r[2], r[3], o); tr2<2>(r[4], r[5], o); tr2<3>(r[6], r[7], o); }
__device__ __forceinline__ v4u seal(v4f a, u32 tag) {
    return v4u{__builtin_amdgcn_perm(asu(a[0]), tag, 0x07060500u), __builtin_amdgcn_perm(asu(a[1]), tag, 0x07060501u),
               __builtin_amdgcn_perm(asu(a[2]), tag, 0x07060502u), __builtin_amdgcn_perm(asu(a[3]), tag, 0x07060503u)};
}
__device__ __forceinline__ v4u tag_bytes(u32 t) { return v4u{t & 255u, (t >> 8) & 255u, (t >> 16) & 255u, t >> 24}; }
__device__ __forceinline__ u32 or4(v4u y) { return y.x | y.y | y.z | y.w; }

#define STAMP(i)
#define STAMPV(i, x)
#define DUMP()

__device__ __forceinline__ void ln_stat(float s1, float s2, float& M, float& r) {
    M = s1 * (1.f / 448);
    const float V = fmaxf(s2 * (1.f / 448) - M * M, 0.f);
    r = __builtin_amdgcn_rcpf(rb(__builtin_amdgcn_sqrtf(rb(rb(V) + 1e-5f))));
    M = rb(M);
}
template <int QB, int JJ> __device__ __forceinline__ u32 bank_mov(u32 old, u32 P) {
    constexpr int C = QB == JJ ? 0xE4 : QB > JJ ? 0x110 + 4 * (QB - JJ) : 0x100 + 4 * (JJ - QB);
    return (u32)__builtin_amdgcn_update_dpp((int)old, (int)P, C, 0xf, 1 << QB, false);
}
template <int QB> __device__ __forceinline__ void bank4(v4u& A, u32 P) {
    A[0] = bank_mov<QB, 0>(A[0], P); A[1] = bank_mov<QB, 1>(A[1], P); A[2] = bank_mov<QB, 2>(A[2], P); A[3] = bank_mov<QB, 3>(A[3], P);
}
template <int QB> __device__ __forceinline__ void bank7(v4u& A, v4u& A2, u32 P0, u32 P1) {
    A[0] = bank_mov<QB, 0>(A[0], P0); A[1] = bank_mov<QB, 0>(A[1], P1); A[2] = bank_mov<QB, 1>(A[2], P0); A[3] = bank_mov<QB, 1>(A[3], P1);
    A2[0] = bank_mov<QB, 2>(A2[0], P0); A2[1] = bank_mov<QB, 2>(A2[1], P1); A2[2] = bank_mov<QB, 3>(A2[2], P1);
}
__device__ __forceinline__ void zero4(v4u& A) { A = v4u{0, 0, 0, 0}; asm volatile("" : "+v"(A)); }
__device__ __forceinline__ float fold(v4f a) {
    return asf((u32)__builtin_amdgcn_update_dpp((int)asu(a[0]), (int)asu(a[2]), ROR8, 0xf, 0xc, false)) +
           asf((u32)__builtin_amdgcn_update_dpp((int)asu(a[3]), (int)asu(a[1]), ROR8, 0xf, 0x3, false));
}
__device__ __forceinline__ v4f gemv(u32 P, v4u* A, const v4u* B) {
    bank4<0>(A[0], P); bank4<1>(A[1], P);
    v4f c[4];
#pragma unroll
    for (int s = 0; s < 4; ++s) c[s] = mfma(A[0], B[s], v4f{0, 0, 0, 0});
#pragma unroll
    for (int s = 0; s < 4; ++s) c[s] = mfma(A[1], B[4 + s], c[s]);
    return v4f{fold(c[0]), fold(c[1]), fold(c[2]), fold(c[3])};
}
__device__ __forceinline__ void until(u64 tz, u32 d) {
    const u32 t0 = (u32)tz;
    while ((u32)__builtin_amdgcn_s_memrealtime() - t0 < d) __builtin_amdgcn_s_sleep(1);
}
constexpr int win(int n) { return n == 2 ? 2 : n < WSW ? WA : WB; }

__global__ void __launch_bounds__(704, 3)
adaln_block_kernel(const char*, const char*, const char*, const char*, const char*, const char*, char*, const char*, const char*,
                   const char*, char*) {
    __shared__ __attribute__((aligned(16))) char lds[L_END];
    const char *xin, *kc, *vc;
    char* ws;
    u32 bid = blockIdx.x, tid, w64, io;
    v8u kb;
    u64 tz;
    asm volatile(R"(
	s_load_dwordx16 s[36:51], s[0:1], 0x0
	s_getpc_b64 s[20:21]
.Ladaln_pc:
	s_lshr_b32 s18, s2, 3
	s_cmp_eq_u32 s18, )" STR(PFI) R"(
	s_cselect_b32 s22, max(.Ladaln_e0-.Ladaln_pc, .Ladaln_e1-.Ladaln_pc), 0
	s_mov_b32 s23, 0x20000
	v_xor_b32 v1, 0x80, v0
	v_lshlrev_b32 v1, 7, v1
	buffer_load_dword v167, v1, s[20:23], 0 offen
	s_memrealtime s[92:93]
	s_load_dwordx8 s[84:91], s[0:1], 0x38
	v_readfirstlane_b32 s17, v0
	v_bfe_u32 v2, v0, 3, 3
	v_and_b32 v3, 7, v0
	s_mov_b32 s22, -1
	s_cmp_gt_u32 s17, 64
	s_cbranch_scc1 .Ladaln_st
	s_setprio 2
	s_and_b32 s18, s2, 3
	s_mulk_i32 s18, 112
	s_lshr_b32 s19, s2, 2
	s_lshl_b32 s19, s19, 7
	v_mad_u32_u24 v2, v2, 14, s18
	v_lshl_add_u32 v3, v3, 4, s19
	s_movk_i32 s52, 0x2000
	s_cmp_eq_u32 s17, 0
	s_cmovk_i32 s52, 5376
	v_mad_u32_u24 v2, v2, s52, v3
	s_add_u32 s53, s52, s52
	s_add_u32 s54, s53, s52
	s_add_u32 s55, s54, s52
	s_add_u32 s56, s55, s52
	s_add_u32 s57, s56, s52
	s_add_u32 s58, s57, s52
	s_add_u32 s59, s58, s52
	s_add_u32 s60, s59, s52
	s_add_u32 s61, s60, s52
	s_add_u32 s62, s61, s52
	s_add_u32 s63, s62, s52
	s_add_u32 s64, s63, s52
	v_and_b32 v4, 12, v0
	v_min_u32 v4, 10, v4
	v_bfe_u32 v5, v0, 4, 2
	v_mad_u32_u24 v4, v5, 28, v4
	v_and_b32 v3, 1, v0
	v_mad_u32_u24 v4, v3, 14, v4
	s_movk_i32 s26, 224
	v_and_b32 v5, 2, v0
	v_mad_u32_u24 v4, v5, s26, v4
	v_add_lshl_u32 v4, v4, s18, 1
	s_movk_i32 s30, 1792
	s_mov_b32 s31, s23
	s_add_u32 s19, s17, s2
	s_cmp_ge_u32 s19, 56
	s_cbranch_scc1 .Ladaln_slow
	s_waitcnt lgkmcnt(0)
	s_load_dword s94, s[48:49], 0x0
	s_mov_b64 s[20:21], s[36:37]
	s_mov_b64 s[28:29], s[40:41]
.Ladaln_go:
	buffer_load_dwordx2 v[110:111], v4, s[28:31], 0 offen
	buffer_load_dwordx4 v[112:115], v2, s[20:23], 0 offen nt
	buffer_load_dwordx4 v[116:119], v2, s[20:23], s52 offen nt
	buffer_load_dwordx4 v[120:123], v2, s[20:23], s53 offen nt
	buffer_load_dwordx4 v[124:127], v2, s[20:23], s54 offen nt
	buffer_load_dwordx4 v[128:131], v2, s[20:23], s55 offen nt
	buffer_load_dwordx4 v[132:135], v2, s[20:23], s56 offen nt
	buffer_load_dwordx4 v[136:139], v2, s[20:23], s57 offen nt
	buffer_load_dwordx4 v[140:143], v2, s[20:23], s58 offen nt
	buffer_load_dwordx4 v[144:147], v2, s[20:23], s59 offen nt
	buffer_load_dwordx4 v[148:151], v2, s[20:23], s60 offen nt
	buffer_load_dwordx4 v[152:155], v2, s[20:23], s61 offen nt
	buffer_load_dwordx4 v[156:159], v2, s[20:23], s62 offen nt
	buffer_load_dwordx4 v[160:163], v2, s[20:23], s63 offen nt
	buffer_load_dwordx4 v[164:167], v2, s[20:23], s64 offen nt
	s_branch .Ladaln_end
.Ladaln_slow:
	v_bfe_u32 v1, v0, 2, 4
	v_mul_u32_u24 v1, 28, v1
	v_mad_u32_u24 v1, v3, 14, v1
	v_mad_u32_u24 v1, v5, s26, v1
	v_lshlrev_b32 v1, 1, v1
	s_cmp_eq_u32 s17, 0
	s_waitcnt lgkmcnt(0)
	s_load_dword s94, s[48:49], 0x0
	s_cselect_b64 s[20:21], s[36:37], s[38:39]
	s_cselect_b64 s[28:29], s[40:41], s[42:43]
	s_cbranch_scc1 .Ladaln_x2
	buffer_load_dwordx4 v[102:105], v1, s[28:31], 0 offen
	buffer_load_dwordx3 v[106:108], v1, s[28:31], 0 offen offset:16
	s_sleep )" STR(QD) R"(
	s_branch .Ladaln_go
.Ladaln_x2:
	s_cmpk_lt_u32 s2, 168
	s_cbranch_scc0 .Ladaln_idle
	s_barrier
	s_barrier
	s_branch .Ladaln_go
.Ladaln_idle:
	s_endpgm
.Ladaln_st:
	s_and_b32 s18, s2, 15
	s_lshl_b32 s18, s18, 6
	s_lshr_b32 s19, s2, 4
	s_add_u32 s18, s18, s19
	s_lshl_b32 s18, s18, 12
	s_add_u32 s18, s18, s17
	s_lshl_b32 s18, s18, 4
	s_add_u32 s18, s18, 0x2ff400
	v_lshlrev_b32 v2, 4, v2
	v_lshl_add_u32 v2, v3, 13, v2
	s_cmpk_eq_u32 s17, 128
	s_waitcnt lgkmcnt(0)
	s_load_dword s94, s[48:49], 0x0
	s_cbranch_scc1 .Ladaln_end
	s_mov_b64 s[20:21], s[44:45]
	s_sleep )" STR(SD) R"(
	buffer_load_dwordx4 v[112:115], v2, s[20:23], s18 offen nt
	buffer_load_dwordx4 v[116:119], v2, s[20:23], s18 offen offset:128 nt
.Ladaln_end:
)"  : "={s[42:43]}"(xin), "={s[44:45]}"(kc), "={s[46:47]}"(vc), "={s[48:49]}"(ws), "+{s2}"(bid), "={s17}"(w64), "={s[84:91]}"(kb), "={s[92:93]}"(tz)
    : "{s[0:1]}"(__builtin_amdgcn_kernarg_segment_ptr()), "{v0}"(threadIdx.x)
    : "memory", "scc", "s18", "s19", "s20", "s21", "s22", "s23", "s26", "s28", "s29", "s30", "s31", "s36", "s37", "s38", "s39", "s40", "s41", "s50", "s51",
      "s52", "s53", "s54", "s55", "s56", "s57", "s58", "s59", "s60", "s61", "s62", "s63", "s64", "s94", "v1", "v2", "v3", "v5", "v102", "v167");
    v4u R[14], xs0;
    v3u xs1;
    v2u in;
    u32 tag;
    asm volatile("" : "={s94}"(tag));
    asm volatile("" : "={v0}"(tid), "={v4}"(io), "={v[110:111]}"(in), "={v[102:105]}"(xs0), "={v[106:108]}"(xs1),
                 "={v[112:115]}"(R[0]), "={v[116:119]}"(R[1]), "={v[120:123]}"(R[2]), "={v[124:127]}"(R[3]), "={v[128:131]}"(R[4]), "={v[132:135]}"(R[5]), "={v[136:139]}"(R[6]),
                 "={v[140:143]}"(R[7]), "={v[144:147]}"(R[8]), "={v[148:151]}"(R[9]), "={v[152:155]}"(R[10]), "={v[156:159]}"(R[11]), "={v[160:163]}"(R[12]), "={v[164:167]}"(R[13]));
    PIN();
    STAMP(1);
    const int w = w64 >> 6;
    const u32 l = tid & 63, idx = bid >> 3, xc = bid & 7;
    const u32 slab = bid & 15, split = bid >> 4;
    const u32 kq = l >> 4, a8 = l & 7, g8 = l >> 3;
    const bool isW = idx >= 24, isF = bid < 28;
#define rsW RS(ws, WS_END)
#define STAMP0()
#define ARGS() do { asm volatile("s_waitcnt lgkmcnt(0) ; args" : "+{s94}"(tag)); \
                    tag = tag * 1664525u + 1013904223u; STAMP0(); } while (0)
#define TILE2(n, i, r0, r1) PIN(); asm volatile("s_waitcnt vmcnt(" #n ") ; tile " r0 " " r1 : "+{v[" r0 "]}"(R[i]), "+{v[" r1 "]}"(R[i + 1]) :: "memory")

    if (__builtin_expect_with_probability(w < 3, 1, 0.55)) {
    if (__builtin_expect_with_probability(w < 2, 1, 0.9)) {
        v4u B0[8], B1[8], A[4];
        v4f c[4];
#pragma unroll
        for (int i = 0; i < 4; ++i) zero4(A[i]);
#pragma unroll
        for (int j = 0; j < 8; ++j) { B1[j][3] = 0; KEEP(B1[j]); }
        const u32 pl = OFF(l >= 32) + (2 * a8 + kq) * 16;
        if (__builtin_expect_with_probability(w == 0, 1, 0.55)) {
            if (bid >= 56) asm volatile("s_barrier" ::: "memory");
            u32 po = pl + WS_MP + (bid & 3) * 10752 + (bid >> 2) * 256 + (g8 & 1) * MPB;
            KEEP(po);
            asm volatile("s_waitcnt vmcnt(14) ; tile 102:111" : "+{v[110:111]}"(in) :: "memory");
            const u32 P0 = pk(silu(lo16(in.x)), silu(hi16(in.x))), P1 = pk(silu(lo16(in.y)), silu(hi16(in.y)));
            bank7<0>(A[0], A[2], P0, P1); bank7<1>(A[1], A[3], P0, P1);
            TILE2(12, 0, "112:115", "116:119"); tr2<0>(R[0], R[1], B0);
            TILE2(10, 2, "120:123", "124:127"); tr2<1>(R[2], R[3], B0);
            ARGS();
            TILE2(8, 4, "128:131", "132:135"); tr2<2>(R[4], R[5], B0);
            TILE2(6, 6, "136:139", "140:143"); tr2<3>(R[6], R[7], B0);
#pragma unroll
            for (int s = 0; s < 4; ++s) c[s] = mfma(A[0], B0[s], v4f{0, 0, 0, 0});
#pragma unroll
            for (int s = 0; s < 4; ++s) c[s] = mfma(A[1], B0[4 + s], c[s]);
            PIN();
            TILE2(4, 8, "144:147", "148:151"); tr2<0>(R[8], R[9], B1);
            TILE2(2, 10, "152:155", "156:159"); tr2<1>(R[10], R[11], B1);
            TILE2(0, 12, "160:163", "164:167");
            STAMP(2);
            tr2<2>(R[12], R[13], B1);
#pragma unroll
            for (int s = 0; s < 4; ++s) c[s] = mfma(A[2], B1[s], c[s]);
#pragma unroll
            for (int s = 0; s < 4; ++s) c[s] = mfma(A[3], B1[4 + s], c[s]);
            ST_C4(seal(v4f{fold(c[0]), fold(c[1]), fold(c[2]), fold(c[3])}, tag), rsW, po);
            STAMP(4);
            DUMP();
            EXIT();
        }
        asm volatile("s_waitcnt vmcnt(14) ; tile 102:111" : "+{v[110:111]}"(in), "+{v[102:105]}"(xs0), "+{v[106:108]}"(xs1) :: "memory");
        float h[4] = {lo16(in.x), hi16(in.x), lo16(in.y), hi16(in.y)};
        {
            float s1 = 0, s2 = 0;
#pragma unroll
            for (int i = 0; i < 7; ++i) {
                const u32 t = i < 4 ? xs0[i & 3] : xs1[i & 3];
                s1 += lo16(t) + hi16(t);
                s2 += lo16(t) * lo16(t) + hi16(t) * hi16(t);
            }
            s1 = sum_nb(s1); s2 = sum_nb(s2);
            float M, r;
            ln_stat(t32_add(s1, s1), t32_add(s2, s2), M, r);
#pragma unroll
            for (int i = 0; i < 4; ++i) h[i] = rb(rb(h[i] - M) * r);
        }
        asm volatile("" : "+v"(h[0]), "+v"(h[1]), "+v"(h[2]), "+v"(h[3]));
        ARGS();
        TILE2(12, 0, "112:115", "116:119"); tr2<0>(R[0], R[1], B0);
        TILE2(10, 2, "120:123", "124:127"); tr2<1>(R[2], R[3], B0);
        TILE2(8, 4, "128:131", "132:135"); tr2<2>(R[4], R[5], B0);
        TILE2(6, 6, "136:139", "140:143"); tr2<3>(R[6], R[7], B0);
        TILE2(4, 8, "144:147", "148:151"); tr2<0>(R[8], R[9], B1);
        TILE2(2, 10, "152:155", "156:159"); tr2<1>(R[10], R[11], B1);
        PIN();
        u32 po = pl + WS_QP + (bid >> 4) * 4096 + (bid & 3) * 1024 + (bid & 12) * 64 + (g8 & 1) * 65536;
        u32 mo = WS_MP + 2 * io + (l & 2) * ((MPB - 1792) / 2);
        asm volatile("" : "+v"(po), "+v"(mo));
        TILE2(0, 12, "160:163", "164:167"); tr2<2>(R[12], R[13], B1);
        KEEP8(B0); KEEP8(B1);
        v4u tr = tag_bytes(__builtin_amdgcn_alignbit(tag, tag, (io & 4) * 4));
        KEEP(tr);
        PIN();
        STAMP(2);
        until(tz, TM);
        v4u y[8];
        SPIN(SPIN_CAP) {
#pragma unroll
            for (int i = 0; i < 8; ++i) y[i] = LD_C4(rsW, mo + (i >> 2) * 1792, (i & 3) * 10752);
            FENCE();
            u32 bad = 0;
#pragma unroll
            for (int i = 0; i < 8; ++i) { y[i] ^= tr; bad |= or4(y[i]); }
            if (NONE(bad & 255u)) break;
        }
        STAMP(3);
        const v4f sa = (asf4(y[0]) + asf4(y[1])) + (asf4(y[2]) + asf4(y[3])), ca = (asf4(y[4]) + asf4(y[5])) + (asf4(y[6]) + asf4(y[7]));
#pragma unroll
        for (int i = 0; i < 4; ++i) h[i] = rb(h[i] * rb(1.f + rb(ca[i]))) + rb(sa[i]);
        const u32 P0 = pk(h[0], h[1]), P1 = pk(h[2], h[3]);
        bank7<0>(A[0], A[2], P0, P1); bank7<1>(A[1], A[3], P0, P1);
#pragma unroll
        for (int s = 0; s < 4; ++s) c[s] = mfma(A[0], B0[s], v4f{0, 0, 0, 0});
#pragma unroll
        for (int s = 0; s < 4; ++s) c[s] = mfma(A[2], B1[s], c[s]);
#pragma unroll
        for (int s = 0; s < 4; ++s) c[s] = mfma(A[1], B0[4 + s], c[s]);
#pragma unroll
        for (int s = 0; s < 4; ++s) c[s] = mfma(A[3], B1[4 + s], c[s]);
        ST_C4(seal(v4f{fold(c[0]), fold(c[1]), fold(c[2]), fold(c[3])}, tag), rsW, po);
        STAMP(4);
    } else {
        asm volatile("s_setprio 2 ; tile 102:166");
        ARGS();
    }
    const v4u tb = tag_bytes(tag);
    const u32 hd = slab * 2 + w - 1;
    until(tz, TP);
    {
        v4u y[4];
        const u32 qo = WS_QP + hd * 4096 + l * 16;
        SPIN(SPIN_CAP) {
#pragma unroll
            for (int i = 0; i < 4; ++i) y[i] = LD_C4(rsW, qo + i * 1024, 0);
            FENCE();
            u32 bad = 0;
#pragma unroll
            for (int i = 0; i < 4; ++i) { y[i] ^= tb; bad |= or4(y[i]); }
            if (NONE(bad & 255u)) break;
        }
        const v4f q = (asf4(y[0]) + asf4(y[1])) + (asf4(y[2]) + asf4(y[3]));
        *(v2u*)(lds + L_QS + (w - 1) * 640 + l * 8) = v2u{pk(q[0], q[1]), pk(q[2], q[3])};
    }
    STAMP(5);
    BAR();
    BAR();
    {
        const v2f ml = *(const v2f*)(lds + L_ML + (w - 1) * 64 + a8 * 8);
        const float M = max_lo3(ml.x);
        const float f = __builtin_amdgcn_exp2f((ml.x - M) * SC);
        const float L = sum_lo3(f * ml.y);
        ST_C4((v4u{asu(M), tag, asu(L), tag}), rsW, l ? OOB : WS_ST + (hd * 16 + split) * 16);
        float fs[8];
#pragma unroll
        for (int i = 0; i < 8; ++i) { fs[i] = rdl(f, i); KEEP(fs[i]); }
        u32 ra = L_OP + (w - 1) * 512 + (l >> 5) * 1024 + (l & 31) * 16, oa = WS_O + (hd * 16 + split) * 1024 + l * 16;
        asm volatile("" : "+v"(ra), "+v"(oa));
        STAMP(6);
        BAR();
        v4f x[8];
#pragma unroll
        for (int i = 0; i < 8; ++i) x[i] = *(const v4f*)(lds + ra + i * 2048);
        v4f o0 = x[0] * fs[0], o1 = x[1] * fs[1];
#pragma unroll
        for (int i = 2; i < 8; i += 2) { o0 += x[i] * fs[i]; o1 += x[i + 1] * fs[i + 1]; }
        ST_C4(seal(o0 + o1, tag), rsW, oa);
        STAMP(7);
    }
    const u32 ru = bid - 28;
    if (isW) asm volatile("s_barrier" ::: "memory");
    if (w == 2 || ru >= 28) { DUMP(); EXIT(); }
    const u32 rb_ = ru >= 14, cb = ru - rb_ * 14;
    const bool hi8 = l & 8;
    float m0, m1, m2;
    u32 e0, e1;
    u32 xo = rb_ * 896 + cb * 64 + a8 * 8 + kq * 2;
    {
        const u32 xv = __builtin_amdgcn_raw_buffer_load_b16(RS(xin, 1792), xo, 0, 0);
        const u32 mo = WS_MP + rb_ * MPB + 3584 + cb * 128 + (l & 8) * 224 + kq * 10752 + a8 * 16;
        v4u y0, y1;
        SPIN(SPIN_CAP) {
            y0 = LD_C4(rsW, mo, 0) ^ tb;
            y1 = LD_C4(rsW, mo + 3584, 0) ^ tb;
            FENCE();
            if (NONE((or4(y0) | or4(y1)) & 255u)) break;
        }
        const float t0 = rb(rows4(asf4(y0))), t1 = rb(rows4(asf4(y1))), o0 = dpp<ROR8>(t0), o1 = dpp<ROR8>(t1);
        m0 = lo16(xv); m1 = hi8 ? o0 : t0; m2 = hi8 ? t1 : o1;
        e0 = pk(1.f + t1, o0); e1 = dppu<QX1>(e0);
    }
    u32 oa = WS_WP + ru * 8192 + l * 16, ob = oa + 4096, oc = ob;
    v4u ta = tb, tc = tb;
    u32 x1o = OFF(l & 9) + WS_X1 + (rb_ * 224 + cb * 16 + a8 * 2 + kq) * 16, xso = OFF(l & 55) + WS_XS + ru * 16 + l;
    xo = hi8 ? OOB : xo;
    asm volatile("" : "+v"(x1o), "+v"(xso), "+v"(xo), "+v"(e0), "+v"(e1), "+v"(m2));
    const rsrc_t rso = RS(out, 1792);
    STAMP(8);
#pragma unroll 1
    for (int ph = 0;; ++ph) {
        v4f s;
        SPIN(SPIN_CAP) {
            v4u y[8];
#pragma unroll
            for (int i = 0; i < 8; ++i) y[i] = LD_C4(rsW, (i < 4 ? oa : i < 7 ? ob : oc) + (i & 3) * 1024, 0);
            FENCE();
            u32 bad = 0;
#pragma unroll
            for (int i = 0; i < 8; ++i) { y[i] ^= i < 4 ? ta : i < 7 ? tb : tc; bad |= or4(y[i]); }
            s = asf4(y[0]);
#pragma unroll
            for (int i = 1; i < 8; ++i) s += asf4(y[i]);
            KEEP(s);
            if (NONE(bad & 255u)) break;
        }
        if (ph) STAMP(11); else STAMP(9);
        float t = rows4(s);
        t += dpp<ROR8>(t);
        const u32 yb = pk(m0 + rb(m1 * rb(t)), 0.f);
        if (ph) {
            __builtin_amdgcn_raw_buffer_store_b16((u16)yb, rso, xo, 0, 0);
            __builtin_amdgcn_raw_buffer_store_b32(tag, rsW, (ru | l) ? OOB : 0, 0, 16);
            STAMP(12);
            DUMP();
            EXIT();
        }
        ST_C4((v4u{yb | (dppu<QX1>(yb) << 16), e0, e1, tag}), rsW, x1o);
        m0 = lo16(yb);
        float u = sum_lo3(hi8 ? m0 * m0 : m0);
        u = t16_add(u, u);
        u = t32_add(u, u);
        ST_C2((v2u{asu(u), tag}), rsW, xso);
        STAMP(10);
        m1 = m2;
        oa = OOB; ob = WS_FP + ru * 8192 + l * 16; oc = l < 32 ? ob : OOB;
        ta = v4u{0, 0, 0, 0}; tc = l < 32 ? tb : ta;
        asm volatile(".Ladaln_e0:");
    }
    }
    {
        asm volatile("; tile 102:111 120:163");
        const int v = w - 3;
        u32 kvb = ((slab << 6) + 48 + split) << 16;
        u32 ko = a8 * 8192 + g8 * 16 + v * 1024, vo = l * 16 + v * 1024;
        asm volatile("" : "+v"(ko), "+v"(vo));
        KEEPS(kvb);
        v4u K[8], V[8];
        const rsrc_t rsK = RS(kc + kvb, 65536), rsV = RS(vc + kvb, 65536);
#pragma unroll
        for (int i = 2; i < 8; ++i) {
            VMWAIT(win(i) - 1);
            K[i] = LD_NT(rsK, ko + i * 128, 0);
            FENCE();
        }
        asm volatile("; tile 102:167" : "+{v[112:115]}"(R[0]), "+{v[116:119]}"(R[1]), "+{v[164:167]}"(R[13]));
        K[0] = R[0]; K[1] = R[1];
        ARGS();
        const u32 wu = (idx & 7) * 8 + xc, hq = 2 * xc + ((idx >> 2) & 1), gq = idx & 3;
        const rsrc_t rsG = RS(isW ? wo + (size_t)(hq * 256 + (gq & 1) * 4 + (gq >> 1) * 2) * 896 + v * 128 : w1 + (size_t)v * 64 * 3584 + bid * 128,
                              v == 7 ? 0 : isW ? 249 * 896 + 128 : 63 * 3584 + 128);
        u32 gv0 = g8 * (isW ? 16 * 896 : 8 * 3584) + a8 * 16, gv1 = gv0 + (isW ? 128 * 896 : 4 * 3584);
        u32 gs1 = isW ? 896 : 3584, gs2 = isW ? 8 * 896 : 2 * 3584, gs3 = gs1 + gs2;
        u32 msel = ((l >> 4) ^ l) & 1 ? 4 : (l >> 1) & 3;
        u32 qa = L_QS + (l & 1) * 640 + (l & 2) * 8 + kq * 32, wa = L_OP + v * 2048 + l * 16;
        u32 ma = (l & 55) ? wa : L_ML + (l + v) * 8;
        asm volatile("" : "+v"(gv0), "+v"(gv1), "+v"(msel), "+v"(qa), "+v"(wa), "+v"(ma));
        asm volatile("" : "+s"(gs1), "+s"(gs2), "+s"(gs3));
#pragma unroll
        for (int i = 0; i < 8; ++i) {
            VMWAIT(win(8 + i) - 1);
            V[i] = LD_NT(rsV, vo, i * 8192);
            FENCE();
        }
        STAMP(2);
        BAR();
        STAMP(3);
        v4f s0 = {0, 0, 0, 0}, s1 = {0, 0, 0, 0};
        v4u Q[4];
#pragma unroll
        for (int j = 0; j < 4; ++j) Q[j] = *(const v4u*)(lds + qa + j * 128);
        PIN();
#pragma unroll
        for (int j = 0; j < 4; ++j) {
            s0 = mfma(Q[j], K[j], s0);
            s1 = mfma(Q[j], K[4 + j], s1);
        }
        const v4f sg = l < 32 ? s0 : s1;
        const float s = rb(fold(v4f{sg[0], sg[2], sg[1], sg[3]}));
        const float m = x32_max(max_lo3(s));
        const float p = __builtin_amdgcn_exp2f((s - m) * SC);
        float ls = sum_lo3(p);
        ls = t32_add(ls, ls);
        *(v2f*)(lds + ma) = v2f{m, ls};
        BAR();
        STAMP(4);
        v4u G[8], PA[4], BV[8];
        {
            const u32 pp = pk(p, dpp<QX1>(p));
            const u32 x0 = dppu<0x00>(pp), x1 = dppu<0xAA>(pp), y0 = dppu<HMIR>(x0), y1 = dppu<HMIR>(x1);
#pragma unroll
            for (int c = 0; c < 4; ++c) PA[c] = msel == c ? (c < 2 ? v4u{x0, x1, y0, y1} : v4u{y0, y1, x0, x1}) : v4u{0, 0, 0, 0};
        }
#define PV(mark) do { \
        tr2<0>(V[0], V[1], BV); tr2<1>(V[2], V[3], BV); tr2<2>(V[4], V[5], BV); \
        asm volatile(mark : "+v"(PA[0]), "+v"(PA[1]), "+v"(PA[2]), "+v"(PA[3])); \
        KEEP8(BV); \
        PIN(); \
        STAMPV(5, V[7]); \
        tr2<3>(V[6], V[7], BV); \
        v4f o0 = {0, 0, 0, 0}, o1 = {0, 0, 0, 0}; \
        _Pragma("unroll") for (int c = 0; c < 4; ++c) { \
            o0 = mfma(PA[c], BV[(c >> 1) * 4 + (c & 1)], o0); \
            o1 = mfma(PA[c], BV[(c >> 1) * 4 + (c & 1) + 2], o1); \
        } \
        *(v4f*)(lds + wa) = o0; \
        *(v4f*)(lds + wa + 1024) = o1; \
        asm volatile("s_waitcnt lgkmcnt(0)\n\ts_barrier " mark ::: "memory"); } while (0)
        ANY8(BV);
        if (!isW && !isF) {
            PV("; a");
            STAMP(6);
            DUMP();
            EXIT();
        }
#pragma unroll
        for (int i = 0; i < 8; ++i) G[i] = LD_NT(rsG, (i & 1) ? gv1 : gv0, i < 2 ? 0 : i < 4 ? gs1 : i < 6 ? gs2 : gs3);
        FENCE(); PIN();
        PV("; b");
        STAMP(6);
        v4u A[2];
        zero4(A[0]); zero4(A[1]);
        const v4u tb = tag_bytes(tag);
        u32 go = OFF(l >= 32) + (isW ? WS_WP + wu * 128 : WS_FP + bid * 128) + (((l >> 3) & 1) * 14 + 2 * v + (a8 >> 2)) * 8192 + ((2 * a8 + kq) & 7) * 16;
        u32 ra = L_P1 + (l & 2) * 192 + kq * 64 + (l & 1) * 32 + (l & 12) * 2;
        asm volatile("" : "+v"(go), "+v"(ra));
        u32 hv;
        if (isW) {
            const u32 sb = ((v >> 2) * 16 + hq) * 16, sp = (v & 3) * 4 + kq;
            v4u st;
            SPIN(SPIN_CAP) {
                st = LD_C4(rsW, WS_ST + (sb + (l & 15)) * 16, 0);
                FENCE();
                if (NONE((st.y ^ tag) | (st.w ^ tag))) break;
            }
            float f = __builtin_amdgcn_exp2f((asf(st.x) - max_row(asf(st.x))) * SC);
            f *= __builtin_amdgcn_rcpf(sum_row(f * asf(st.z)));
            f = asf((u32)__builtin_amdgcn_ds_bpermute(sp * 4, (int)asu(f)));
            tr8(G, BV);
            u32 oo = WS_O + (sb + sp) * 1024 + gq * 256 + (l & 15) * 16;
            u32 pa = L_P1 + (v & 3) * 2064 + (v >> 2) * 384 + (l & 15) * 16 + kq * 4;
            asm volatile("" : "+v"(f), "+v"(oo), "+v"(pa));
            KEEP8(BV);
            PIN();
            STAMP(7);
            v4u y;
            SPIN(SPIN_CAP) {
                y = LD_C4(rsW, oo, 0) ^ tb;
                FENCE();
                if (NONE(or4(y) & 255u)) break;
            }
            STAMP(8);
            *(float*)(lds + pa) = rows4(asf4(y) * f);
            BAR();
            if (v == 7) { DUMP(); EXIT(); }
            const v2f t = (*(const v2f*)(lds + ra) + *(const v2f*)(lds + ra + 2064)) + (*(const v2f*)(lds + ra + 2 * 2064) + *(const v2f*)(lds + ra + 3 * 2064));
            hv = pk(t.x, t.y);
        } else {
            if (v == 7) { DUMP(); EXIT(); }
            const rsrc_t rs2 = RS(w2 + (size_t)bid * 57344 + v * 128, 63 * 896 + 128);
            const u32 v2 = g8 * 7168 + a8 * 16;
#pragma unroll
            for (int i = 0; i < 8; ++i) V[i] = LD_NT(rs2, v2 + (i & 3) * 896, (i >> 2) * 3584);
            FENCE();
            v4u B1[8];
            tr8(G, B1);
            tr8(V, BV);
            const u32 si = (l & 16) / 2 + (l & 12) / 2 + (l & 1);
            const u32 ts_ = si < 14 ? tag : 0u;
            u32 xo = WS_X1 + ((l & 2) * 112 + v * 32 + kq * 8 + (l & 1) * 4 + ((l >> 2) & 3)) * 16;
            u32 so = OFF(si >= 14) + WS_XS + ((l & 2) * 7 + si) * 16;
            u32 pa = L_P1 + v * 2064 + (l & 8) * 48 + (2 * a8 + (kq & 1)) * 16 + (l & 32) * 32;
            asm volatile("" : "+v"(xo), "+v"(so), "+v"(pa));
            KEEP8(B1);
            KEEP8(BV);
            PIN();
            STAMP(7);
            v4u xg, sg2;
            SPIN(SPIN_CAP) {
                xg = LD_C4(rsW, xo, 0);
                sg2 = LD_C4(rsW, so, 0);
                FENCE();
                if (NONE((xg.w ^ tag) | (sg2.y ^ ts_) | (sg2.w ^ ts_))) break;
            }
            STAMP(8);
            float M, r;
            ln_stat(sum_nb(asf(sg2.x)), sum_nb(asf(sg2.z)), M, r);
            float a0 = lo16(xg.x) - M, a1 = hi16(xg.x) - M;
            rb2(a0, a1);
            a0 *= r; a1 *= r;
            rb2(a0, a1);
            a0 *= lo16(xg.y); a1 *= lo16(xg.z);
            rb2(a0, a1);
            const v4f t1 = gemv(pk(a0 + hi16(xg.y), a1 + hi16(xg.z)), A, B1);
            *(v4f*)(lds + pa) = t1;
            BAR();
            STAMP(9);
            v2f x[7];
#pragma unroll
            for (int i = 0; i < 7; ++i) x[i] = *(const v2f*)(lds + ra + i * 2064);
            const v2f t = ((x[0] + x[1]) + (x[2] + x[3])) + ((x[4] + x[5]) + x[6]);
            hv = pk(t.x, t.y);
            hv = pk(silu(lo16(hv)), silu(hi16(hv)));
        }
        ST_C4(seal(gemv(hv, A, BV), tag), rsW, go);
        STAMP(10);
        asm volatile(".Ladaln_e1:");
        DUMP();
        EXIT();
    }

}

extern "C" void kernel_launch(void* const* d_in, const int* in_sizes, int n_in, void* d_out, int out_size, void* d_ws, size_t ws_size,
                              hipStream_t stream) {
    adaln_block_kernel<<<256, 704, 0, stream>>>((const char*)d_in[9], (const char*)d_in[3], (const char*)d_in[8], (const char*)d_in[0],
                                                (const char*)d_in[1], (const char*)d_in[2], (char*)d_ws, (const char*)d_in[4],
                                                (const char*)d_in[6], (const char*)d_in[7], (char*)d_out);
}
```

```cpp
#include <hip/hip_runtime.h>

typedef unsigned u32;
typedef unsigned long long u64;
typedef unsigned short u16;
typedef u32 v4u __attribute__((ext_vector_type(4)));
typedef u32 v3u __attribute__((ext_vector_type(3)));
typedef u32 v2u __attribute__((ext_vector_type(2)));
typedef float v4f __attribute__((ext_vector_type(4)));
typedef float v2f __attribute__((ext_vector_type(2)));
typedef u32 v8u __attribute__((ext_vector_type(8)));
typedef u32 v16u __attribute__((ext_vector_type(16)));
typedef __bf16 v8bf __attribute__((ext_vector_type(8)));
typedef __bf16 v2bf __attribute__((ext_vector_type(2)));
typedef __amdgpu_buffer_rsrc_t rsrc_t;

#define WA 3
#define WB 3
#define WSW 10
#define QD 8
#define SD 24
#define TM 230
#define TP 450
#define PFI 7
#define STR_(x) #x
#define STR(x) STR_(x)

constexpr u32 WS_MP = 2097152;
constexpr u32 MPB = 43008;
constexpr u32 WS_ST = 98304;
constexpr u32 WS_X1 = 106496;
constexpr u32 WS_XS = 114688;
constexpr u32 WS_QP = 1310720;
constexpr u32 WS_WP = 262144;
constexpr u32 WS_FP = 491520;
constexpr u32 WS_O = 720896;
constexpr u32 WS_END = WS_MP + 2 * MPB;
constexpr u32 OOB = 0x40000000u;
#define SPIN_CAP_V 400000u
constexpr u32 SPIN_CAP = SPIN_CAP_V;

constexpr int L_ML = 0;
constexpr int L_QS = 128;
constexpr int L_OP = L_QS + 1280;
constexpr int L_P1 = L_OP + 16384;
constexpr int L_END = L_P1 + 7 * 2064;

#define RS(p, n) __builtin_amdgcn_make_buffer_rsrc((void*)(p), (short)0, (int)(n), 0x00020000)
#define LD_NT(rs, vo, so) __builtin_amdgcn_raw_buffer_load_b128(rs, (int)(vo), (int)(so), 2)
#define LD_C4(rs, vo, so) __builtin_amdgcn_raw_buffer_load_b128(rs, (int)(vo), (int)(so), 16)
#define ST_C4(v, rs, vo) __builtin_amdgcn_raw_buffer_store_b128(v, rs, (int)(vo), 0, 16)
#define ST_C2(v, rs, vo) __builtin_amdgcn_raw_buffer_store_b64(v, rs, (int)(vo), 0, 16)
#define FENCE() asm volatile("" ::: "memory")
#define ARG(v, i) ((char*)(((u64)v[2 * (i) + 1] << 32) | v[2 * (i)]))
#define wo ARG(kb, 0)
#define w1 ARG(kb, 1)
#define w2 ARG(kb, 2)
#define out ARG(kb, 3)
#define EXIT() __builtin_amdgcn_endpgm()
#define KEEP(x) asm volatile("" : "+v"(x))
#define KEEPS(x) asm volatile("" : "+s"(x))
#define KEEP8(x) asm volatile("" : "+v"(x[0]), "+v"(x[1]), "+v"(x[2]), "+v"(x[3]), "+v"(x[4]), "+v"(x[5]), "+v"(x[6]), "+v"(x[7]))
#define ANY8(x) asm volatile("" : "=v"(x[0]), "=v"(x[1]), "=v"(x[2]), "=v"(x[3]), "=v"(x[4]), "=v"(x[5]), "=v"(x[6]), "=v"(x[7]))
__device__ __forceinline__ bool spin_more(u32& n) { asm volatile("" : "+s"(n)); return --n != 0; }
#define SPIN(n) _Pragma("unroll 1") for (u32 sp_ = (n), go_ = 1; go_; go_ = spin_more(sp_))
#define NONE(bad) (__builtin_amdgcn_ballot_w64((bad) != 0u) == 0ull)
#define OFF(c) ((c) ? OOB : 0u)
#define PIN() __builtin_amdgcn_sched_barrier(0)
#define BAR() do { asm volatile("s_waitcnt lgkmcnt(0)\n\ts_barrier" ::: "memory"); } while (0)
#define VMWAIT(n) asm volatile("s_waitcnt vmcnt(%0)" :: "i"(n) : "memory")

__device__ __forceinline__ float asf(u32 x) { return __builtin_bit_cast(float, x); }
__device__ __forceinline__ u32 asu(float x) { return __builtin_bit_cast(u32, x); }
__device__ __forceinline__ v4f asf4(v4u x) { return __builtin_bit_cast(v4f, x); }
__device__ __forceinline__ float lo16(u32 x) { return asf(x << 16); }
__device__ __forceinline__ float hi16(u32 x) { return asf(x & 0xffff0000u); }
__device__ __forceinline__ u32 pk(float a, float b) { v2bf v = {(__bf16)a, (__bf16)b}; return __builtin_bit_cast(u32, v); }
__device__ __forceinline__ float rb(float a) { return (float)(__bf16)a; }
__device__ __forceinline__ void rb2(float& a, float& b) { const u32 t = pk(a, b); a = lo16(t); b = hi16(t); }
template <int C> __device__ __forceinline__ float dpp(float x) { return asf((u32)__builtin_amdgcn_update_dpp(0, (int)asu(x), C, 0xf, 0xf, true)); }
template <int C> __device__ __forceinline__ u32 dppu(u32 x) { return (u32)__builtin_amdgcn_update_dpp(0, (int)x, C, 0xf, 0xf, true); }
constexpr int QX1 = 0xB1, QX2 = 0x4E, HMIR = 0x141, ROR4 = 0x124, ROR8 = 0x128;
__device__ __forceinline__ float t32_add(float a, float b) { auto s = __builtin_amdgcn_permlane32_swap(asu(a), asu(b), false, false); return asf(s[0]) + asf(s[1]); }
__device__ __forceinline__ float t16_add(float a, float b) { auto s = __builtin_amdgcn_permlane16_swap(asu(a), asu(b), false, false); return asf(s[0]) + asf(s[1]); }
__device__ __forceinline__ float fmx(float a, float b) { return __builtin_fmaxf(a, b); }
__device__ __forceinline__ float x32_max(float x) { auto s = __builtin_amdgcn_permlane32_swap(asu(x), asu(x), false, false); return fmx(asf(s[0]), asf(s[1])); }
__device__ __forceinline__ float rows4(v4f a) { return t16_add(t32_add(a[0], a[2]), t32_add(a[1], a[3])); }
__device__ __forceinline__ float sum_lo3(float x) { x += dpp<QX1>(x); x += dpp<QX2>(x); x += dpp<HMIR>(x); return x; }
__device__ __forceinline__ float max_lo3(float x) { x = fmx(x, dpp<QX1>(x)); x = fmx(x, dpp<QX2>(x)); x = fmx(x, dpp<HMIR>(x)); return x; }
__device__ __forceinline__ float sum_row(float x) { x = sum_lo3(x); return x + dpp<ROR8>(x); }
__device__ __forceinline__ float max_row(float x) { x = max_lo3(x); return fmx(x, dpp<ROR8>(x)); }
__device__ __forceinline__ float sum_nb(float x) { x += dpp<QX1>(x); x += dpp<ROR4>(x); x += dpp<ROR8>(x); return t16_add(x, x); }
__device__ __forceinline__ v4f mfma(v4u a, v4u b, v4f c) {
    return __builtin_amdgcn_mfma_f32_16x16x32_bf16(__builtin_bit_cast(v8bf, a), __builtin_bit_cast(v8bf, b), c, 0, 0, 0);
}
__device__ __forceinline__ float silu(float v) { return v * __builtin_amdgcn_rcpf(1.f + __builtin_amdgcn_exp2f(v * -1.44269504089f)); }
__device__ __forceinline__ float rdl(float v, int lane) { return asf((u32)__builtin_amdgcn_readlane((int)asu(v), lane)); }
constexpr float SC = 0.0625f * 1.44269504089f;

template <int T> __device__ __forceinline__ void tr2(const v4u& r0, const v4u& r1, v4u* o) {
#pragma unroll
    for (int j = 0; j < 8; ++j) o[j][T] = __builtin_amdgcn_perm(r1[j >> 1], r0[j >> 1], (j & 1) ? 0x07060302u : 0x05040100u);
}
__device__ __forceinline__ void tr8(const v4u* r, v4u* o) { tr2<0>(r[0], r[1], o); tr2<1>(r[2], r[3], o); tr2<2>(r[4], r[5], o); tr2<3>(r[6], r[7], o); }
__device__ __forceinline__ v4u seal(v4f a, u32 tag) {
    return v4u{__builtin_amdgcn_perm(asu(a[0]), tag, 0x07060500u), __builtin_amdgcn_perm(asu(a[1]), tag, 0x07060501u),
               __builtin_amdgcn_perm(asu(a[2]), tag, 0x07060502u), __builtin_amdgcn_perm(asu(a[3]), tag, 0x07060503u)};
}
__device__ __forceinline__ v4u tag_bytes(u32 t) { return v4u{t & 255u, (t >> 8) & 255u, (t >> 16) & 255u, t >> 24}; }
__device__ __forceinline__ u32 or4(v4u y) { return y.x | y.y | y.z | y.w; }

#define STAMP(i)
#define STAMPV(i, x)
#define DUMP()

__device__ __forceinline__ void ln_stat(float s1, float s2, float& M, float& r) {
    M = s1 * (1.f / 448);
    const float V = fmaxf(s2 * (1.f / 448) - M * M, 0.f);
    r = __builtin_amdgcn_rcpf(rb(__builtin_amdgcn_sqrtf(rb(rb(V) + 1e-5f))));
    M = rb(M);
}
template <int QB, int JJ> __device__ __forceinline__ u32 bank_mov(u32 old, u32 P) {
    constexpr int C = QB == JJ ? 0xE4 : QB > JJ ? 0x110 + 4 * (QB - JJ) : 0x100 + 4 * (JJ - QB);
    return (u32)__builtin_amdgcn_update_dpp((int)old, (int)P, C, 0xf, 1 << QB, false);
}
template <int QB> __device__ __forceinline__ void bank4(v4u& A, u32 P) {
    A[0] = bank_mov<QB, 0>(A[0], P); A[1] = bank_mov<QB, 1>(A[1], P); A[2] = bank_mov<QB, 2>(A[2], P); A[3] = bank_mov<QB, 3>(A[3], P);
}
template <int QB> __device__ __forceinline__ void bank7(v4u& A, v4u& A2, u32 P0, u32 P1) {
    A[0] = bank_mov<QB, 0>(A[0], P0); A[1] = bank_mov<QB, 0>(A[1], P1); A[2] = bank_mov<QB, 1>(A[2], P0); A[3] = bank_mov<QB, 1>(A[3], P1);
    A2[0] = bank_mov<QB, 2>(A2[0], P0); A2[1] = bank_mov<QB, 2>(A2[1], P1); A2[2] = bank_mov<QB, 3>(A2[2], P1);
}
__device__ __forceinline__ void zero4(v4u& A) { A = v4u{0, 0, 0, 0}; asm volatile("" : "+v"(A)); }
__device__ __forceinline__ float fold(v4f a) {
    return asf((u32)__builtin_amdgcn_update_dpp((int)asu(a[0]), (int)asu(a[2]), ROR8, 0xf, 0xc, false)) +
           asf((u32)__builtin_amdgcn_update_dpp((int)asu(a[3]), (int)asu(a[1]), ROR8, 0xf, 0x3, false));
}
__device__ __forceinline__ v4f gemv(u32 P, v4u* A, const v4u* B) {
    bank4<0>(A[0], P); bank4<1>(A[1], P);
    v4f c[4];
#pragma unroll
    for (int s = 0; s < 4; ++s) c[s] = mfma(A[0], B[s], v4f{0, 0, 0, 0});
#pragma unroll
    for (int s = 0; s < 4; ++s) c[s] = mfma(A[1], B[4 + s], c[s]);
    return v4f{fold(c[0]), fold(c[1]), fold(c[2]), fold(c[3])};
}
__device__ __forceinline__ void until(u64 tz, u32 d) {
    const u32 t0 = (u32)tz;
    while ((u32)__builtin_amdgcn_s_memrealtime() - t0 < d) __builtin_amdgcn_s_sleep(1);
}
constexpr int win(int n) { return n == 2 ? 2 : n < WSW ? WA : WB; }

__global__ void __launch_bounds__(704, 3)
adaln_block_kernel(const char*, const char*, const char*, const char*, const char*, const char*, char*, const char*, const char*,
                   const char*, char*) {
    __shared__ __attribute__((aligned(16))) char lds[L_END];
    const char *xin, *kc, *vc;
    char* ws;
    u32 bid = blockIdx.x, tid, w64, io;
    v8u kb;
    u64 tz;
    asm volatile(R"(
	s_load_dwordx16 s[36:51], s[0:1], 0x0
	s_getpc_b64 s[20:21]
.Ladaln_pc:
	s_lshr_b32 s18, s2, 3
	s_cmp_eq_u32 s18, )" STR(PFI) R"(
	s_cselect_b32 s22, max(.Ladaln_e0-.Ladaln_pc, .Ladaln_e1-.Ladaln_pc), 0
	s_mov_b32 s23, 0x20000
	v_xor_b32 v1, 0x80, v0
	v_lshlrev_b32 v1, 7, v1
	buffer_load_dword v167, v1, s[20:23], 0 offen
	s_memrealtime s[92:93]
	s_load_dwordx8 s[84:91], s[0:1], 0x38
	v_readfirstlane_b32 s17, v0
	v_bfe_u32 v2, v0, 3, 3
	v_and_b32 v3, 7, v0
	s_mov_b32 s22, -1
	s_cmp_gt_u32 s17, 64
	s_cbranch_scc1 .Ladaln_st
	s_setprio 2
	s_and_b32 s18, s2, 3
	s_mulk_i32 s18, 112
	s_lshr_b32 s19, s2, 2
	s_lshl_b32 s19, s19, 7
	v_mad_u32_u24 v2, v2, 14, s18
	v_lshl_add_u32 v3, v3, 4, s19
	s_movk_i32 s52, 0x2000
	s_cmp_eq_u32 s17, 0
	s_cmovk_i32 s52, 5376
	v_mad_u32_u24 v2, v2, s52, v3
	s_add_u32 s53, s52, s52
	s_add_u32 s54, s53, s52
	s_add_u32 s55, s54, s52
	s_add_u32 s56, s55, s52
	s_add_u32 s57, s56, s52
	s_add_u32 s58, s57, s52
	s_add_u32 s59, s58, s52
	s_add_u32 s60, s59, s52
	s_add_u32 s61, s60, s52
	s_add_u32 s62, s61, s52
	s_add_u32 s63, s62, s52
	s_add_u32 s64, s63, s52
	v_and_b32 v4, 12, v0
	v_min_u32 v4, 10, v4
	v_bfe_u32 v5, v0, 4, 2
	v_mad_u32_u24 v4, v5, 28, v4
	v_and_b32 v3, 1, v0
	v_mad_u32_u24 v4, v3, 14, v4
	s_movk_i32 s26, 224
	v_and_b32 v5, 2, v0
	v_mad_u32_u24 v4, v5, s26, v4
	v_add_lshl_u32 v4, v4, s18, 1
	s_movk_i32 s30, 1792
	s_mov_b32 s31, s23
	s_add_u32 s19, s17, s2
	s_cmp_ge_u32 s19, 56
	s_cbranch_scc1 .Ladaln_slow
	s_waitcnt lgkmcnt(0)
	s_load_dword s94, s[48:49], 0x0
	s_mov_b64 s[20:21], s[36:37]
	s_mov_b64 s[28:29], s[40:41]
.Ladaln_go:
	buffer_load_dwordx2 v[110:111], v4, s[28:31], 0 offen
	buffer_load_dwordx4 v[112:115], v2, s[20:23], 0 offen nt
	buffer_load_dwordx4 v[116:119], v2, s[20:23], s52 offen nt
	buffer_load_dwordx4 v[120:123], v2, s[20:23], s53 offen nt
	buffer_load_dwordx4 v[124:127], v2, s[20:23], s54 offen nt
	buffer_load_dwordx4 v[128:131], v2, s[20:23], s55 offen nt
	buffer_load_dwordx4 v[132:135], v2, s[20:23], s56 offen nt
	buffer_load_dwordx4 v[136:139], v2, s[20:23], s57 offen nt
	buffer_load_dwordx4 v[140:143], v2, s[20:23], s58 offen nt
	buffer_load_dwordx4 v[144:147], v2, s[20:23], s59 offen nt
	buffer_load_dwordx4 v[148:151], v2, s[20:23], s60 offen nt
	buffer_load_dwordx4 v[152:155], v2, s[20:23], s61 offen nt
	buffer_load_dwordx4 v[156:159], v2, s[20:23], s62 offen nt
	buffer_load_dwordx4 v[160:163], v2, s[20:23], s63 offen nt
	buffer_load_dwordx4 v[164:167], v2, s[20:23], s64 offen nt
	s_branch .Ladaln_end
.Ladaln_slow:
	v_bfe_u32 v1, v0, 2, 4
	v_mul_u32_u24 v1, 28, v1
	v_mad_u32_u24 v1, v3, 14, v1
	v_mad_u32_u24 v1, v5, s26, v1
	v_lshlrev_b32 v1, 1, v1
	s_cmp_eq_u32 s17, 0
	s_waitcnt lgkmcnt(0)
	s_load_dword s94, s[48:49], 0x0
	s_cselect_b64 s[20:21], s[36:37], s[38:39]
	s_cselect_b64 s[28:29], s[40:41], s[42:43]
	s_cbranch_scc1 .Ladaln_x2
	buffer_load_dwordx4 v[102:105], v1, s[28:31], 0 offen
	buffer_load_dwordx3 v[106:108], v1, s[28:31], 0 offen offset:16
	s_sleep )" STR(QD) R"(
	s_branch .Ladaln_go
.Ladaln_x2:
	s_cmpk_lt_u32 s2, 168
	s_cbranch_scc0 .Ladaln_idle
	s_waitcnt lgkmcnt(0)
	s_barrier
	s_barrier
	s_branch .Ladaln_go
.Ladaln_idle:
	s_endpgm
.Ladaln_st:
	s_and_b32 s18, s2, 15
	s_lshl_b32 s18, s18, 6
	s_lshr_b32 s19, s2, 4
	s_add_u32 s18, s18, s19
	s_lshl_b32 s18, s18, 12
	s_add_u32 s18, s18, s17
	s_lshl_b32 s18, s18, 4
	s_add_u32 s18, s18, 0x2ff400
	v_lshlrev_b32 v2, 4, v2
	v_lshl_add_u32 v2, v3, 13, v2
	s_cmpk_eq_u32 s17, 128
	s_waitcnt lgkmcnt(0)
	s_load_dword s94, s[48:49], 0x0
	s_cbranch_scc1 .Ladaln_end
	s_mov_b64 s[20:21], s[44:45]
	s_sleep )" STR(SD) R"(
	buffer_load_dwordx4 v[112:115], v2, s[20:23], s18 offen nt
	buffer_load_dwordx4 v[116:119], v2, s[20:23], s18 offen offset:128 nt
.Ladaln_end:
)"  : "={s[42:43]}"(xin), "={s[44:45]}"(kc), "={s[46:47]}"(vc), "={s[48:49]}"(ws), "+{s2}"(bid), "={s17}"(w64), "={s[84:91]}"(kb), "={s[92:93]}"(tz)
    : "{s[0:1]}"(__builtin_amdgcn_kernarg_segment_ptr()), "{v0}"(threadIdx.x)
    : "memory", "scc", "s18", "s19", "s20", "s21", "s22", "s23", "s26", "s28", "s29", "s30", "s31", "s36", "s37", "s38", "s39", "s40", "s41", "s50", "s51",
      "s52", "s53", "s54", "s55", "s56", "s57", "s58", "s59", "s60", "s61", "s62", "s63", "s64", "s94", "v1", "v2", "v3", "v5", "v102", "v167");
    v4u R[14], xs0;
    v3u xs1;
    v2u in;
    u32 tag;
    asm volatile("" : "={s94}"(tag));
    asm volatile("" : "={v0}"(tid), "={v4}"(io), "={v[110:111]}"(in), "={v[102:105]}"(xs0), "={v[106:108]}"(xs1),
                 "={v[112:115]}"(R[0]), "={v[116:119]}"(R[1]), "={v[120:123]}"(R[2]), "={v[124:127]}"(R[3]), "={v[128:131]}"(R[4]), "={v[132:135]}"(R[5]), "={v[136:139]}"(R[6]),
                 "={v[140:143]}"(R[7]), "={v[144:147]}"(R[8]), "={v[148:151]}"(R[9]), "={v[152:155]}"(R[10]), "={v[156:159]}"(R[11]), "={v[160:163]}"(R[12]), "={v[164:167]}"(R[13]));
    PIN();
    STAMP(1);
    const int w = w64 >> 6;
    const u32 l = tid & 63, idx = bid >> 3, xc = bid & 7;
    const u32 slab = bid & 15, split = bid >> 4;
    const u32 kq = l >> 4, a8 = l & 7, g8 = l >> 3;
    const bool isW = idx >= 24, isF = bid < 28;
#define rsW RS(ws, WS_END)
#define STAMP0()
#define ARGS() do { asm volatile("s_waitcnt lgkmcnt(0) ; args" : "+{s94}"(tag)); \
                    tag = tag * 1664525u + 1013904223u; STAMP0(); } while (0)
#define TILE2(n, i, r0, r1) PIN(); asm volatile("s_waitcnt vmcnt(" #n ") ; tile " r0 " " r1 : "+{v[" r0 "]}"(R[i]), "+{v[" r1 "]}"(R[i + 1]) :: "memory")

    if (__builtin_expect_with_probability(w < 3, 1, 0.55)) {
    if (__builtin_expect_with_probability(w < 2, 1, 0.9)) {
        v4u B0[8], B1[8], A[4];
        v4f c[4];
#pragma unroll
        for (int i = 0; i < 4; ++i) zero4(A[i]);
#pragma unroll
        for (int j = 0; j < 8; ++j) { B1[j][3] = 0; KEEP(B1[j]); }
        const u32 pl = OFF(l >= 32) + (2 * a8 + kq) * 16;
        if (__builtin_expect_with_probability(w == 0, 1, 0.55)) {
            if (bid >= 56) asm volatile("s_barrier" ::: "memory");
            u32 po = pl + WS_MP + (bid & 3) * 10752 + (bid >> 2) * 256 + (g8 & 1) * MPB;
            KEEP(po);
            asm volatile("s_waitcnt vmcnt(14) ; tile 102:111" : "+{v[110:111]}"(in) :: "memory");
            const u32 P0 = pk(silu(lo16(in.x)), silu(hi16(in.x))), P1 = pk(silu(lo16(in.y)), silu(hi16(in.y)));
            bank7<0>(A[0], A[2], P0, P1); bank7<1>(A[1], A[3], P0, P1);
            TILE2(12, 0, "112:115", "116:119"); tr2<0>(R[0], R[1], B0);
            TILE2(10, 2, "120:123", "124:127"); tr2<1>(R[2], R[3], B0);
            ARGS();
            TILE2(8, 4, "128:131", "132:135"); tr2<2>(R[4], R[5], B0);
            TILE2(6, 6, "136:139", "140:143"); tr2<3>(R[6], R[7], B0);
#pragma unroll
            for (int s = 0; s < 4; ++s) c[s] = mfma(A[0], B0[s], v4f{0, 0, 0, 0});
#pragma unroll
            for (int s = 0; s < 4; ++s) c[s] = mfma(A[1], B0[4 + s], c[s]);
            PIN();
            TILE2(4, 8, "144:147", "148:151"); tr2<0>(R[8], R[9], B1);
            TILE2(2, 10, "152:155", "156:159"); tr2<1>(R[10], R[11], B1);
            TILE2(0, 12, "160:163", "164:167");
            STAMP(2);
            tr2<2>(R[12], R[13], B1);
#pragma unroll
            for (int s = 0; s < 4; ++s) c[s] = mfma(A[2], B1[s], c[s]);
#pragma unroll
            for (int s = 0; s < 4; ++s) c[s] = mfma(A[3], B1[4 + s], c[s]);
            ST_C4(seal(v4f{fold(c[0]), fold(c[1]), fold(c[2]), fold(c[3])}, tag), rsW, po);
            STAMP(4);
            DUMP();
            EXIT();
        }
        asm volatile("s_waitcnt vmcnt(14) ; tile 102:111" : "+{v[110:111]}"(in), "+{v[102:105]}"(xs0), "+{v[106:108]}"(xs1) :: "memory");
        float h[4] = {lo16(in.x), hi16(in.x), lo16(in.y), hi16(in.y)};
        {
            float s1 = 0, s2 = 0;
#pragma unroll
            for (int i = 0; i < 7; ++i) {
                const u32 t = i < 4 ? xs0[i & 3] : xs1[i & 3];
                s1 += lo16(t) + hi16(t);
                s2 += lo16(t) * lo16(t) + hi16(t) * hi16(t);
            }
            s1 = sum_nb(s1); s2 = sum_nb(s2);
            float M, r;
            ln_stat(t32_add(s1, s1), t32_add(s2, s2), M, r);
#pragma unroll
            for (int i = 0; i < 4; ++i) h[i] = rb(rb(h[i] - M) * r);
        }
        asm volatile("" : "+v"(h[0]), "+v"(h[1]), "+v"(h[2]), "+v"(h[3]));
        ARGS();
        TILE2(12, 0, "112:115", "116:119"); tr2<0>(R[0], R[1], B0);
        TILE2(10, 2, "120:123", "124:127"); tr2<1>(R[2], R[3], B0);
        TILE2(8, 4, "128:131", "132:135"); tr2<2>(R[4], R[5], B0);
        TILE2(6, 6, "136:139", "140:143"); tr2<3>(R[6], R[7], B0);
        TILE2(4, 8, "144:147", "148:151"); tr2<0>(R[8], R[9], B1);
        TILE2(2, 10, "152:155", "156:159"); tr2<1>(R[10], R[11], B1);
        PIN();
        u32 po = pl + WS_QP + (bid >> 4) * 4096 + (bid & 3) * 1024 + (bid & 12) * 64 + (g8 & 1) * 65536;
        u32 mo = WS_MP + 2 * io + (l & 2) * ((MPB - 1792) / 2);
        asm volatile("" : "+v"(po), "+v"(mo));
        TILE2(0, 12, "160:163", "164:167"); tr2<2>(R[12], R[13], B1);
        KEEP8(B0); KEEP8(B1);
        v4u tr = tag_bytes(__builtin_amdgcn_alignbit(tag, tag, (io & 4) * 4));
        KEEP(tr);
        PIN();
        STAMP(2);
        until(tz, TM);
        v4u y[8];
        SPIN(SPIN_CAP) {
#pragma unroll
            for (int i = 0; i < 8; ++i) y[i] = LD_C4(rsW, mo + (i >> 2) * 1792, (i & 3) * 10752);
            FENCE();
            u32 bad = 0;
#pragma unroll
            for (int i = 0; i < 8; ++i) { y[i] ^= tr; bad |= or4(y[i]); }
            if (NONE(bad & 255u)) break;
        }
        STAMP(3);
        const v4f sa = (asf4(y[0]) + asf4(y[1])) + (asf4(y[2]) + asf4(y[3])), ca = (asf4(y[4]) + asf4(y[5])) + (asf4(y[6]) + asf4(y[7]));
#pragma unroll
        for (int i = 0; i < 4; ++i) h[i] = rb(h[i] * rb(1.f + rb(ca[i]))) + rb(sa[i]);
        const u32 P0 = pk(h[0], h[1]), P1 = pk(h[2], h[3]);
        bank7<0>(A[0], A[2], P0, P1); bank7<1>(A[1], A[3], P0, P1);
#pragma unroll
        for (int s = 0; s < 4; ++s) c[s] = mfma(A[0], B0[s], v4f{0, 0, 0, 0});
#pragma unroll
        for (int s = 0; s < 4; ++s) c[s] = mfma(A[2], B1[s], c[s]);
#pragma unroll
        for (int s = 0; s < 4; ++s) c[s] = mfma(A[1], B0[4 + s], c[s]);
#pragma unroll
        for (int s = 0; s < 4; ++s) c[s] = mfma(A[3], B1[4 + s], c[s]);
        ST_C4(seal(v4f{fold(c[0]), fold(c[1]), fold(c[2]), fold(c[3])}, tag), rsW, po);
        STAMP(4);
    } else {
        asm volatile("s_setprio 2 ; tile 102:166");
        ARGS();
    }
    const v4u tb = tag_bytes(tag);
    const u32 hd = slab * 2 + w - 1;
    until(tz, TP);
    {
        v4u y[4];
        const u32 qo = WS_QP + hd * 4096 + l * 16;
        SPIN(SPIN_CAP) {
#pragma unroll
            for (int i = 0; i < 4; ++i) y[i] = LD_C4(rsW, qo + i * 1024, 0);
            FENCE();
            u32 bad = 0;
#pragma unroll
            for (int i = 0; i < 4; ++i) { y[i] ^= tb; bad |= or4(y[i]); }
            if (NONE(bad & 255u)) break;
        }
        const v4f q = (asf4(y[0]) + asf4(y[1])) + (asf4(y[2]) + asf4(y[3]));
        *(v2u*)(lds + L_QS + (w - 1) * 640 + l * 8) = v2u{pk(q[0], q[1]), pk(q[2], q[3])};
    }
    STAMP(5);
    BAR();
    BAR();
    {
        const v2f ml = *(const v2f*)(lds + L_ML + (w - 1) * 64 + a8 * 8);
        const float M = max_lo3(ml.x);
        const float f = __builtin_amdgcn_exp2f((ml.x - M) * SC);
        const float L = sum_lo3(f * ml.y);
        ST_C4((v4u{asu(M), tag, asu(L), tag}), rsW, l ? OOB : WS_ST + (hd * 16 + split) * 16);
        float fs[8];
#pragma unroll
        for (int i = 0; i < 8; ++i) { fs[i] = rdl(f, i); KEEP(fs[i]); }
        u32 ra = L_OP + (w - 1) * 512 + (l >> 5) * 1024 + (l & 31) * 16, oa = WS_O + (hd * 16 + split) * 1024 + l * 16;
        asm volatile("" : "+v"(ra), "+v"(oa));
        STAMP(6);
        BAR();
        v4f x[8];
#pragma unroll
        for (int i = 0; i < 8; ++i) x[i] = *(const v4f*)(lds + ra + i * 2048);
        v4f o0 = x[0] * fs[0], o1 = x[1] * fs[1];
#pragma unroll
        for (int i = 2; i < 8; i += 2) { o0 += x[i] * fs[i]; o1 += x[i + 1] * fs[i + 1]; }
        ST_C4(seal(o0 + o1, tag), rsW, oa);
        STAMP(7);
    }
    const u32 ru = bid - 28;
    if (isW) asm volatile("s_barrier" ::: "memory");
    if (w == 2 || ru >= 28) { DUMP(); EXIT(); }
    const u32 rb_ = ru >= 14, cb = ru - rb_ * 14;
    const bool hi8 = l & 8;
    float m0, m1, m2;
    u32 e0, e1;
    u32 xo = rb_ * 896 + cb * 64 + a8 * 8 + kq * 2;
    {
        const u32 xv = __builtin_amdgcn_raw_buffer_load_b16(RS(xin, 1792), xo, 0, 0);
        const u32 mo = WS_MP + rb_ * MPB + 3584 + cb * 128 + (l & 8) * 224 + kq * 10752 + a8 * 16;
        v4u y0, y1;
        SPIN(SPIN_CAP) {
            y0 = LD_C4(rsW, mo, 0) ^ tb;
            y1 = LD_C4(rsW, mo + 3584, 0) ^ tb;
            FENCE();
            if (NONE((or4(y0) | or4(y1)) & 255u)) break;
        }
        const float t0 = rb(rows4(asf4(y0))), t1 = rb(rows4(asf4(y1))), o0 = dpp<ROR8>(t0), o1 = dpp<ROR8>(t1);
        m0 = lo16(xv); m1 = hi8 ? o0 : t0; m2 = hi8 ? t1 : o1;
        e0 = pk(1.f + t1, o0); e1 = dppu<QX1>(e0);
    }
    u32 oa = WS_WP + ru * 8192 + l * 16, ob = oa + 4096, oc = ob;
    v4u ta = tb, tc = tb;
    u32 x1o = OFF(l & 9) + WS_X1 + (rb_ * 224 + cb * 16 + a8 * 2 + kq) * 16, xso = OFF(l & 55) + WS_XS + ru * 16 + l;
    xo = hi8 ? OOB : xo;
    asm volatile("" : "+v"(x1o), "+v"(xso), "+v"(xo), "+v"(e0), "+v"(e1), "+v"(m2));
    const rsrc_t rso = RS(out, 1792);
    { u32 tch = __builtin_amdgcn_raw_buffer_load_b16(rso, xo, 0, 16); KEEP(tch); }
    STAMP(8);
#pragma unroll 1
    for (int ph = 0;; ++ph) {
        v4f s;
        SPIN(SPIN_CAP) {
            v4u y[8];
#pragma unroll
            for (int i = 0; i < 8; ++i) y[i] = LD_C4(rsW, (i < 4 ? oa : i < 7 ? ob : oc) + (i & 3) * 1024, 0);
            FENCE();
            u32 bad = 0;
#pragma unroll
            for (int i = 0; i < 8; ++i) { y[i] ^= i < 4 ? ta : i < 7 ? tb : tc; bad |= or4(y[i]); }
            s = asf4(y[0]);
#pragma unroll
            for (int i = 1; i < 8; ++i) s += asf4(y[i]);
            KEEP(s);
            if (NONE(bad & 255u)) break;
        }
        if (ph) STAMP(11); else STAMP(9);
        float t = rows4(s);
        t += dpp<ROR8>(t);
        const u32 yb = pk(m0 + rb(m1 * rb(t)), 0.f);
        if (ph) {
            until(tz, 1800);
            __builtin_amdgcn_raw_buffer_store_b16((u16)yb, rso, xo, 0, 16);
            STAMP(12);
            DUMP();
            EXIT();
        }
        ST_C4((v4u{yb | (dppu<QX1>(yb) << 16), e0, e1, tag}), rsW, x1o);
        m0 = lo16(yb);
        float u = sum_lo3(hi8 ? m0 * m0 : m0);
        u = t16_add(u, u);
        u = t32_add(u, u);
        ST_C2((v2u{asu(u), tag}), rsW, xso);
        __builtin_amdgcn_raw_buffer_store_b32(tag, rsW, (ru | l) ? OOB : 0, 0, 16);
        STAMP(10);
        m1 = m2;
        oa = OOB; ob = WS_FP + ru * 8192 + l * 16; oc = l < 32 ? ob : OOB;
        ta = v4u{0, 0, 0, 0}; tc = l < 32 ? tb : ta;
        asm volatile(".Ladaln_e0:");
    }
    }
    {
        asm volatile("; tile 102:111 120:163");
        const int v = w - 3;
        u32 kvb = ((slab << 6) + 48 + split) << 16;
        u32 ko = a8 * 8192 + g8 * 16 + v * 1024, vo = l * 16 + v * 1024;
        asm volatile("" : "+v"(ko), "+v"(vo));
        KEEPS(kvb);
        v4u K[8], V[8];
        const rsrc_t rsK = RS(kc + kvb, 65536), rsV = RS(vc + kvb, 65536);
#pragma unroll
        for (int i = 2; i < 8; ++i) {
            VMWAIT(win(i) - 1);
            K[i] = LD_NT(rsK, ko + i * 128, 0);
            FENCE();
        }
        asm volatile("; tile 102:167" : "+{v[112:115]}"(R[0]), "+{v[116:119]}"(R[1]), "+{v[164:167]}"(R[13]));
        K[0] = R[0]; K[1] = R[1];
        ARGS();
        const u32 wu = (idx & 7) * 8 + xc, hq = 2 * xc + ((idx >> 2) & 1), gq = idx & 3;
        const rsrc_t rsG = RS(isW ? wo + (size_t)(hq * 256 + (gq & 1) * 4 + (gq >> 1) * 2) * 896 + v * 128 : w1 + (size_t)v * 64 * 3584 + bid * 128,
                              v == 7 ? 0 : isW ? 249 * 896 + 128 : 63 * 3584 + 128);
        u32 gv0 = g8 * (isW ? 16 * 896 : 8 * 3584) + a8 * 16, gv1 = gv0 + (isW ? 128 * 896 : 4 * 3584);
        u32 gs1 = isW ? 896 : 3584, gs2 = isW ? 8 * 896 : 2 * 3584, gs3 = gs1 + gs2;
        u32 msel = ((l >> 4) ^ l) & 1 ? 4 : (l >> 1) & 3;
        u32 qa = L_QS + (l & 1) * 640 + (l & 2) * 8 + kq * 32, wa = L_OP + v * 2048 + l * 16;
        u32 ma = (l & 55) ? wa : L_ML + (l + v) * 8;
        asm volatile("" : "+v"(gv0), "+v"(gv1), "+v"(msel), "+v"(qa), "+v"(wa), "+v"(ma));
        asm volatile("" : "+s"(gs1), "+s"(gs2), "+s"(gs3));
#pragma unroll
        for (int i = 0; i < 8; ++i) {
            VMWAIT(win(8 + i) - 1);
            V[i] = LD_NT(rsV, vo, i * 8192);
            FENCE();
        }
        STAMP(2);
        BAR();
        STAMP(3);
        v4f s0 = {0, 0, 0, 0}, s1 = {0, 0, 0, 0};
        v4u Q[4];
#pragma unroll
        for (int j = 0; j < 4; ++j) Q[j] = *(const v4u*)(lds + qa + j * 128);
        PIN();
#pragma unroll
        for (int j = 0; j < 4; ++j) {
            s0 = mfma(Q[j], K[j], s0);
            s1 = mfma(Q[j], K[4 + j], s1);
        }
        const v4f sg = l < 32 ? s0 : s1;
        const float s = rb(fold(v4f{sg[0], sg[2], sg[1], sg[3]}));
        const float m = x32_max(max_lo3(s));
        const float p = __builtin_amdgcn_exp2f((s - m) * SC);
        float ls = sum_lo3(p);
        ls = t32_add(ls, ls);
        *(v2f*)(lds + ma) = v2f{m, ls};
        BAR();
        STAMP(4);
        v4u G[8], PA[4], BV[8];
        {
            const u32 pp = pk(p, dpp<QX1>(p));
            const u32 x0 = dppu<0x00>(pp), x1 = dppu<0xAA>(pp), y0 = dppu<HMIR>(x0), y1 = dppu<HMIR>(x1);
#pragma unroll
            for (int c = 0; c < 4; ++c) PA[c] = msel == c ? (c < 2 ? v4u{x0, x1, y0, y1} : v4u{y0, y1, x0, x1}) : v4u{0, 0, 0, 0};
        }
#define PV(mark) do { \
        tr2<0>(V[0], V[1], BV); tr2<1>(V[2], V[3], BV); tr2<2>(V[4], V[5], BV); \
        asm volatile(mark : "+v"(PA[0]), "+v"(PA[1]), "+v"(PA[2]), "+v"(PA[3])); \
        KEEP8(BV); \
        PIN(); \
        STAMPV(5, V[7]); \
        tr2<3>(V[6], V[7], BV); \
        v4f o0 = {0, 0, 0, 0}, o1 = {0, 0, 0, 0}; \
        _Pragma("unroll") for (int c = 0; c < 4; ++c) { \
            o0 = mfma(PA[c], BV[(c >> 1) * 4 + (c & 1)], o0); \
            o1 = mfma(PA[c], BV[(c >> 1) * 4 + (c & 1) + 2], o1); \
        } \
        *(v4f*)(lds + wa) = o0; \
        *(v4f*)(lds + wa + 1024) = o1; \
        asm volatile("s_waitcnt lgkmcnt(0)\n\ts_barrier " mark ::: "memory"); } while (0)
        ANY8(BV);
        if (!isW && !isF) {
            PV("; a");
            STAMP(6);
            DUMP();
            EXIT();
        }
#pragma unroll
        for (int i = 0; i < 8; ++i) G[i] = LD_NT(rsG, (i & 1) ? gv1 : gv0, i < 2 ? 0 : i < 4 ? gs1 : i < 6 ? gs2 : gs3);
        FENCE(); PIN();
        PV("; b");
        STAMP(6);
        v4u A[2];
        zero4(A[0]); zero4(A[1]);
        const v4u tb = tag_bytes(tag);
        u32 go = OFF(l >= 32) + (isW ? WS_WP + wu * 128 : WS_FP + bid * 128) + (((l >> 3) & 1) * 14 + 2 * v + (a8 >> 2)) * 8192 + ((2 * a8 + kq) & 7) * 16;
        u32 ra = L_P1 + (l & 2) * 192 + kq * 64 + (l & 1) * 32 + (l & 12) * 2;
        asm volatile("" : "+v"(go), "+v"(ra));
        u32 hv;
        if (isW) {
            const u32 sb = ((v >> 2) * 16 + hq) * 16, sp = (v & 3) * 4 + kq;
            v4u st;
            SPIN(SPIN_CAP) {
                st = LD_C4(rsW, WS_ST + (sb + (l & 15)) * 16, 0);
                FENCE();
                if (NONE((st.y ^ tag) | (st.w ^ tag))) break;
            }
            float f = __builtin_amdgcn_exp2f((asf(st.x) - max_row(asf(st.x))) * SC);
            f *= __builtin_amdgcn_rcpf(sum_row(f * asf(st.z)));
            f = asf((u32)__builtin_amdgcn_ds_bpermute(sp * 4, (int)asu(f)));
            tr8(G, BV);
            u32 oo = WS_O + (sb + sp) * 1024 + gq * 256 + (l & 15) * 16;
            u32 pa = L_P1 + (v & 3) * 2064 + (v >> 2) * 384 + (l & 15) * 16 + kq * 4;
            asm volatile("" : "+v"(f), "+v"(oo), "+v"(pa));
            KEEP8(BV);
            PIN();
            STAMP(7);
            v4u y;
            SPIN(SPIN_CAP) {
                y = LD_C4(rsW, oo, 0) ^ tb;
                FENCE();
                if (NONE(or4(y) & 255u)) break;
            }
            STAMP(8);
            *(float*)(lds + pa) = rows4(asf4(y) * f);
            BAR();
            if (v == 7) { DUMP(); EXIT(); }
            const v2f t = (*(const v2f*)(lds + ra) + *(const v2f*)(lds + ra + 2064)) + (*(const v2f*)(lds + ra + 2 * 2064) + *(const v2f*)(lds + ra + 3 * 2064));
            hv = pk(t.x, t.y);
        } else {
            if (v == 7) { DUMP(); EXIT(); }
            const rsrc_t rs2 = RS(w2 + (size_t)bid * 57344 + v * 128, 63 * 896 + 128);
            const u32 v2 = g8 * 7168 + a8 * 16;
#pragma unroll
            for (int i = 0; i < 8; ++i) V[i] = LD_NT(rs2, v2 + (i & 3) * 896, (i >> 2) * 3584);
            FENCE();
            v4u B1[8];
            tr8(G, B1);
            tr8(V, BV);
            const u32 si = (l & 16) / 2 + (l & 12) / 2 + (l & 1);
            const u32 ts_ = si < 14 ? tag : 0u;
            u32 xo = WS_X1 + ((l & 2) * 112 + v * 32 + kq * 8 + (l & 1) * 4 + ((l >> 2) & 3)) * 16;
            u32 so = OFF(si >= 14) + WS_XS + ((l & 2) * 7 + si) * 16;
            u32 pa = L_P1 + v * 2064 + (l & 8) * 48 + (2 * a8 + (kq & 1)) * 16 + (l & 32) * 32;
            asm volatile("" : "+v"(xo), "+v"(so), "+v"(pa));
            KEEP8(B1);
            KEEP8(BV);
            PIN();
            STAMP(7);
            v4u xg, sg2;
            SPIN(SPIN_CAP) {
                xg = LD_C4(rsW, xo, 0);
                sg2 = LD_C4(rsW, so, 0);
                FENCE();
                if (NONE((xg.w ^ tag) | (sg2.y ^ ts_) | (sg2.w ^ ts_))) break;
            }
            STAMP(8);
            float M, r;
            ln_stat(sum_nb(asf(sg2.x)), sum_nb(asf(sg2.z)), M, r);
            float a0 = lo16(xg.x) - M, a1 = hi16(xg.x) - M;
            rb2(a0, a1);
            a0 *= r; a1 *= r;
            rb2(a0, a1);
            a0 *= lo16(xg.y); a1 *= lo16(xg.z);
            rb2(a0, a1);
            const v4f t1 = gemv(pk(a0 + hi16(xg.y), a1 + hi16(xg.z)), A, B1);
            *(v4f*)(lds + pa) = t1;
            BAR();
            STAMP(9);
            v2f x[7];
#pragma unroll
            for (int i = 0; i < 7; ++i) x[i] = *(const v2f*)(lds + ra + i * 2064);
            const v2f t = ((x[0] + x[1]) + (x[2] + x[3])) + ((x[4] + x[5]) + x[6]);
            hv = pk(t.x, t.y);
            hv = pk(silu(lo16(hv)), silu(hi16(hv)));
        }
        ST_C4(seal(gemv(hv, A, BV), tag), rsW, go);
        STAMP(10);
        asm volatile(".Ladaln_e1:");
        DUMP();
        EXIT();
    }

}

extern "C" void kernel_launch(void* const* d_in, const int* in_sizes, int n_in, void* d_out, int out_size, void* d_ws, size_t ws_size,
                              hipStream_t stream) {
    adaln_block_kernel<<<256, 704, 0, stream>>>((const char*)d_in[9], (const char*)d_in[3], (const char*)d_in[8], (const char*)d_in[0],
                                                (const char*)d_in[1], (const char*)d_in[2], (char*)d_ws, (const char*)d_in[4],
                                                (const char*)d_in[6], (const char*)d_in[7], (char*)d_out);
}
```

```cpp
#include <hip/hip_runtime.h>

typedef unsigned u32;
typedef unsigned long long u64;
typedef unsigned short u16;
typedef u32 v4u __attribute__((ext_vector_type(4)));
typedef u32 v3u __attribute__((ext_vector_type(3)));
typedef u32 v2u __attribute__((ext_vector_type(2)));
typedef float v4f __attribute__((ext_vector_type(4)));
typedef float v2f __attribute__((ext_vector_type(2)));
typedef u32 v8u __attribute__((ext_vector_type(8)));
typedef u32 v16u __attribute__((ext_vector_type(16)));
typedef __bf16 v8bf __attribute__((ext_vector_type(8)));
typedef __bf16 v2bf __attribute__((ext_vector_type(2)));
typedef __amdgpu_buffer_rsrc_t rsrc_t;

#define WA 3
#define WB 3
#define WSW 10
#define QD 8
#define SD 24
#define TM 230
#define TP 450
#define RD 2
#define RP 0
#define R2D 2
#define R2P 0
#define FD 1
#define FP_ 0
#define WD 1
#define WP_ 0
#define PFI 7
#define STR_(x) #x
#define STR(x) STR_(x)

constexpr u32 WS_MP = 2097152;
constexpr u32 MPB = 43008;
constexpr u32 WS_ST = 196608;
constexpr u32 WS_X1 = 106496;
constexpr u32 WS_XE = 114688;
constexpr u32 WS_QP = 1310720;
constexpr u32 WS_WP = 262144;
constexpr u32 WS_FP = 491520;
constexpr u32 WS_O = 720896;
constexpr u32 WS_END = WS_MP + 2 * MPB;
constexpr u32 OOB = 0x40000000u;
#define SPIN_CAP_V 400000u
constexpr u32 SPIN_CAP = SPIN_CAP_V;

constexpr int L_ML = 0;
constexpr int L_QS = 128;
constexpr int L_OP = L_QS + 1280;
constexpr int L_P1 = L_OP + 16384;
constexpr int L_END = L_P1 + 7 * 2064;

#define RS(p, n) __builtin_amdgcn_make_buffer_rsrc((void*)(p), (short)0, (int)(n), 0x00020000)
#define LD_NT(rs, vo, so) __builtin_amdgcn_raw_buffer_load_b128(rs, (int)(vo), (int)(so), 2)
#define LD_C4(rs, vo, so) __builtin_amdgcn_raw_buffer_load_b128(rs, (int)(vo), (int)(so), 16)
#define ST_C4(v, rs, vo) __builtin_amdgcn_raw_buffer_store_b128(v, rs, (int)(vo), 0, 16)
#define ST_C2(v, rs, vo) __builtin_amdgcn_raw_buffer_store_b64(v, rs, (int)(vo), 0, 16)
#define FENCE() asm volatile("" ::: "memory")
#define ARG(v, i) ((char*)(((u64)v[2 * (i) + 1] << 32) | v[2 * (i)]))
#define wo ARG(kb, 0)
#define w1 ARG(kb, 1)
#define w2 ARG(kb, 2)
#define out ARG(kb, 3)
#define EXIT() __builtin_amdgcn_endpgm()
#define KEEP(x) asm volatile("" : "+v"(x))
#define KEEPS(x) asm volatile("" : "+s"(x))
#define KEEP8(x) asm volatile("" : "+v"(x[0]), "+v"(x[1]), "+v"(x[2]), "+v"(x[3]), "+v"(x[4]), "+v"(x[5]), "+v"(x[6]), "+v"(x[7]))
#define ANY8(x) asm volatile("" : "=v"(x[0]), "=v"(x[1]), "=v"(x[2]), "=v"(x[3]), "=v"(x[4]), "=v"(x[5]), "=v"(x[6]), "=v"(x[7]))
__device__ __forceinline__ bool spin_more(u32& n) { asm volatile("" : "+s"(n)); return --n != 0; }
#define SPIN(n) _Pragma("unroll 1") for (u32 sp_ = (n), go_ = 1; go_; go_ = spin_more(sp_))
#define POLL(D, P, ISSUE, CHECK) do { \
    _Pragma("unroll") for (int d_ = 0; d_ + 1 < D; ++d_) { ISSUE(d_); FENCE(); if (P) __builtin_amdgcn_s_sleep(P); } \
    SPIN(SPIN_CAP) { \
        bool ok_ = false; \
        _Pragma("unroll") for (int d_ = 0; d_ < D; ++d_) { \
            ISSUE((d_ + D - 1) % D); \
            FENCE(); \
            CHECK(d_); \
            if (NONE(bad)) { ok_ = true; break; } \
            if (P) __builtin_amdgcn_s_sleep(P); \
        } \
        if (ok_) break; \
    } } while (0)
#define NONE(bad) (__builtin_amdgcn_ballot_w64((bad) != 0u) == 0ull)
#define OFF(c) ((c) ? OOB : 0u)
#define PIN() __builtin_amdgcn_sched_barrier(0)
#define BAR() do { asm volatile("s_waitcnt lgkmcnt(0)\n\ts_barrier" ::: "memory"); } while (0)
#define VMWAIT(n) asm volatile("s_waitcnt vmcnt(%0)" :: "i"(n) : "memory")

__device__ __forceinline__ float asf(u32 x) { return __builtin_bit_cast(float, x); }
__device__ __forceinline__ u32 asu(float x) { return __builtin_bit_cast(u32, x); }
__device__ __forceinline__ v4f asf4(v4u x) { return __builtin_bit_cast(v4f, x); }
__device__ __forceinline__ float lo16(u32 x) { return asf(x << 16); }
__device__ __forceinline__ float hi16(u32 x) { return asf(x & 0xffff0000u); }
__device__ __forceinline__ u32 pk(float a, float b) { v2bf v = {(__bf16)a, (__bf16)b}; return __builtin_bit_cast(u32, v); }
__device__ __forceinline__ float rb(float a) { return (float)(__bf16)a; }
__device__ __forceinline__ void rb2(float& a, float& b) { const u32 t = pk(a, b); a = lo16(t); b = hi16(t); }
template <int C> __device__ __forceinline__ float dpp(float x) { return asf((u32)__builtin_amdgcn_update_dpp(0, (int)asu(x), C, 0xf, 0xf, true)); }
template <int C> __device__ __forceinline__ u32 dppu(u32 x) { return (u32)__builtin_amdgcn_update_dpp(0, (int)x, C, 0xf, 0xf, true); }
constexpr int QX1 = 0xB1, QX2 = 0x4E, HMIR = 0x141, ROR4 = 0x124, ROR8 = 0x128;
__device__ __forceinline__ float t32_add(float a, float b) { auto s = __builtin_amdgcn_permlane32_swap(asu(a), asu(b), false, false); return asf(s[0]) + asf(s[1]); }
__device__ __forceinline__ float t16_add(float a, float b) { auto s = __builtin_amdgcn_permlane16_swap(asu(a), asu(b), false, false); return asf(s[0]) + asf(s[1]); }
__device__ __forceinline__ float fmx(float a, float b) { return __builtin_fmaxf(a, b); }
__device__ __forceinline__ float x32_max(float x) { auto s = __builtin_amdgcn_permlane32_swap(asu(x), asu(x), false, false); return fmx(asf(s[0]), asf(s[1])); }
__device__ __forceinline__ float rows4(v4f a) { return t16_add(t32_add(a[0], a[2]), t32_add(a[1], a[3])); }
__device__ __forceinline__ float sum_lo3(float x) { x += dpp<QX1>(x); x += dpp<QX2>(x); x += dpp<HMIR>(x); return x; }
__device__ __forceinline__ float max_lo3(float x) { x = fmx(x, dpp<QX1>(x)); x = fmx(x, dpp<QX2>(x)); x = fmx(x, dpp<HMIR>(x)); return x; }
__device__ __forceinline__ float sum_row(float x) { x = sum_lo3(x); return x + dpp<ROR8>(x); }
__device__ __forceinline__ float max_row(float x) { x = max_lo3(x); return fmx(x, dpp<ROR8>(x)); }
__device__ __forceinline__ float sum_nb(float x) { x += dpp<QX1>(x); x += dpp<ROR4>(x); x += dpp<ROR8>(x); return t16_add(x, x); }
__device__ __forceinline__ v4f mfma(v4u a, v4u b, v4f c) {
    return __builtin_amdgcn_mfma_f32_16x16x32_bf16(__builtin_bit_cast(v8bf, a), __builtin_bit_cast(v8bf, b), c, 0, 0, 0);
}
__device__ __forceinline__ float silu(float v) { return v * __builtin_amdgcn_rcpf(1.f + __builtin_amdgcn_exp2f(v * -1.44269504089f)); }
__device__ __forceinline__ float rdl(float v, int lane) { return asf((u32)__builtin_amdgcn_readlane((int)asu(v), lane)); }
constexpr float SC = 0.0625f * 1.44269504089f;

template <int T> __device__ __forceinline__ void tr2(const v4u& r0, const v4u& r1, v4u* o) {
#pragma unroll
    for (int j = 0; j < 8; ++j) o[j][T] = __builtin_amdgcn_perm(r1[j >> 1], r0[j >> 1], (j & 1) ? 0x07060302u : 0x05040100u);
}
__device__ __forceinline__ void tr8(const v4u* r, v4u* o) { tr2<0>(r[0], r[1], o); tr2<1>(r[2], r[3], o); tr2<2>(r[4], r[5], o); tr2<3>(r[6], r[7], o); }
__device__ __forceinline__ v4u seal(v4f a, u32 tag) {
    return v4u{__builtin_amdgcn_perm(asu(a[0]), tag, 0x07060500u), __builtin_amdgcn_perm(asu(a[1]), tag, 0x07060501u),
               __builtin_amdgcn_perm(asu(a[2]), tag, 0x07060502u), __builtin_amdgcn_perm(asu(a[3]), tag, 0x07060503u)};
}
__device__ __forceinline__ v4u tag_bytes(u32 t) { return v4u{t & 255u, (t >> 8) & 255u, (t >> 16) & 255u, t >> 24}; }
__device__ __forceinline__ u32 or4(v4u y) { return y.x | y.y | y.z | y.w; }

#define STAMP(i)
#define STAMPV(i, x)
#define DUMP()

__device__ __forceinline__ void ln_stat(float s1, float s2, float& M, float& r) {
    M = s1 * (1.f / 448);
    const float V = fmaxf(s2 * (1.f / 448) - M * M, 0.f);
    r = __builtin_amdgcn_rcpf(rb(__builtin_amdgcn_sqrtf(rb(rb(V) + 1e-5f))));
    M = rb(M);
}
template <int QB, int JJ> __device__ __forceinline__ u32 bank_mov(u32 old, u32 P) {
    constexpr int C = QB == JJ ? 0xE4 : QB > JJ ? 0x110 + 4 * (QB - JJ) : 0x100 + 4 * (JJ - QB);
    return (u32)__builtin_amdgcn_update_dpp((int)old, (int)P, C, 0xf, 1 << QB, false);
}
template <int QB> __device__ __forceinline__ void bank4(v4u& A, u32 P) {
    A[0] = bank_mov<QB, 0>(A[0], P); A[1] = bank_mov<QB, 1>(A[1], P); A[2] = bank_mov<QB, 2>(A[2], P); A[3] = bank_mov<QB, 3>(A[3], P);
}
template <int QB> __device__ __forceinline__ void bank7(v4u& A, v4u& A2, u32 P0, u32 P1) {
    A[0] = bank_mov<QB, 0>(A[0], P0); A[1] = bank_mov<QB, 0>(A[1], P1); A[2] = bank_mov<QB, 1>(A[2], P0); A[3] = bank_mov<QB, 1>(A[3], P1);
    A2[0] = bank_mov<QB, 2>(A2[0], P0); A2[1] = bank_mov<QB, 2>(A2[1], P1); A2[2] = bank_mov<QB, 3>(A2[2], P1);
}
__device__ __forceinline__ void zero4(v4u& A) { A = v4u{0, 0, 0, 0}; asm volatile("" : "+v"(A)); }
__device__ __forceinline__ float fold(v4f a) {
    return asf((u32)__builtin_amdgcn_update_dpp((int)asu(a[0]), (int)asu(a[2]), ROR8, 0xf, 0xc, false)) +
           asf((u32)__builtin_amdgcn_update_dpp((int)asu(a[3]), (int)asu(a[1]), ROR8, 0xf, 0x3, false));
}
__device__ __forceinline__ v4f gemv(u32 P, v4u* A, const v4u* B) {
    bank4<0>(A[0], P); bank4<1>(A[1], P);
    v4f c[4];
#pragma unroll
    for (int s = 0; s < 4; ++s) c[s] = mfma(A[0], B[s], v4f{0, 0, 0, 0});
#pragma unroll
    for (int s = 0; s < 4; ++s) c[s] = mfma(A[1], B[4 + s], c[s]);
    return v4f{fold(c[0]), fold(c[1]), fold(c[2]), fold(c[3])};
}
__device__ __forceinline__ void until(u64 tz, u32 d) {
    const u32 t0 = (u32)tz;
    while ((u32)__builtin_amdgcn_s_memrealtime() - t0 < d) __builtin_amdgcn_s_sleep(1);
}
constexpr int win(int n) { return n == 2 ? 2 : n < WSW ? WA : WB; }

__global__ void __launch_bounds__(704, 3)
adaln_block_kernel(const char*, const char*, const char*, const char*, const char*, const char*, char*, const char*, const char*,
                   const char*, char*) {
    __shared__ __attribute__((aligned(16))) char lds[L_END];
    const char *xin, *kc, *vc;
    char* ws;
    u32 bid = blockIdx.x, tid, w64, io;
    v8u kb;
    u64 tz;
    asm volatile(R"(
	s_load_dwordx16 s[36:51], s[0:1], 0x0
	s_getpc_b64 s[20:21]
.Ladaln_pc:
	s_lshr_b32 s18, s2, 3
	s_cmp_eq_u32 s18, )" STR(PFI) R"(
	s_cselect_b32 s22, max(.Ladaln_e0-.Ladaln_pc, .Ladaln_e1-.Ladaln_pc), 0
	s_mov_b32 s23, 0x20000
	v_xor_b32 v1, 0x80, v0
	v_lshlrev_b32 v1, 7, v1
	buffer_load_dword v167, v1, s[20:23], 0 offen
	s_memrealtime s[92:93]
	s_load_dwordx8 s[84:91], s[0:1], 0x38
	v_readfirstlane_b32 s17, v0
	v_bfe_u32 v2, v0, 3, 3
	v_and_b32 v3, 7, v0
	s_mov_b32 s22, -1
	s_cmp_gt_u32 s17, 64
	s_cbranch_scc1 .Ladaln_st
	s_setprio 2
	s_and_b32 s18, s2, 3
	s_mulk_i32 s18, 112
	s_lshr_b32 s19, s2, 2
	s_lshl_b32 s19, s19, 7
	v_mad_u32_u24 v2, v2, 14, s18
	v_lshl_add_u32 v3, v3, 4, s19
	s_movk_i32 s52, 0x2000
	s_cmp_eq_u32 s17, 0
	s_cmovk_i32 s52, 5376
	v_mad_u32_u24 v2, v2, s52, v3
	s_add_u32 s53, s52, s52
	s_add_u32 s54, s53, s52
	s_add_u32 s55, s54, s52
	s_add_u32 s56, s55, s52
	s_add_u32 s57, s56, s52
	s_add_u32 s58, s57, s52
	s_add_u32 s59, s58, s52
	s_add_u32 s60, s59, s52
	s_add_u32 s61, s60, s52
	s_add_u32 s62, s61, s52
	s_add_u32 s63, s62, s52
	s_add_u32 s64, s63, s52
	v_and_b32 v4, 12, v0
	v_min_u32 v4, 10, v4
	v_bfe_u32 v5, v0, 4, 2
	v_mad_u32_u24 v4, v5, 28, v4
	v_and_b32 v3, 1, v0
	v_mad_u32_u24 v4, v3, 14, v4
	s_movk_i32 s26, 224
	v_and_b32 v5, 2, v0
	v_mad_u32_u24 v4, v5, s26, v4
	v_add_lshl_u32 v4, v4, s18, 1
	s_movk_i32 s30, 1792
	s_mov_b32 s31, s23
	s_add_u32 s19, s17, s2
	s_cmp_ge_u32 s19, 56
	s_cbranch_scc1 .Ladaln_slow
	s_waitcnt lgkmcnt(0)
	s_load_dword s94, s[48:49], 0x0
	s_mov_b64 s[20:21], s[36:37]
	s_mov_b64 s[28:29], s[40:41]
.Ladaln_go:
	buffer_load_dwordx2 v[110:111], v4, s[28:31], 0 offen
	buffer_load_dwordx4 v[112:115], v2, s[20:23], 0 offen nt
	buffer_load_dwordx4 v[116:119], v2, s[20:23], s52 offen nt
	buffer_load_dwordx4 v[120:123], v2, s[20:23], s53 offen nt
	buffer_load_dwordx4 v[124:127], v2, s[20:23], s54 offen nt
	buffer_load_dwordx4 v[128:131], v2, s[20:23], s55 offen nt
	buffer_load_dwordx4 v[132:135], v2, s[20:23], s56 offen nt
	buffer_load_dwordx4 v[136:139], v2, s[20:23], s57 offen nt
	buffer_load_dwordx4 v[140:143], v2, s[20:23], s58 offen nt
	buffer_load_dwordx4 v[144:147], v2, s[20:23], s59 offen nt
	buffer_load_dwordx4 v[148:151], v2, s[20:23], s60 offen nt
	buffer_load_dwordx4 v[152:155], v2, s[20:23], s61 offen nt
	buffer_load_dwordx4 v[156:159], v2, s[20:23], s62 offen nt
	buffer_load_dwordx4 v[160:163], v2, s[20:23], s63 offen nt
	buffer_load_dwordx4 v[164:167], v2, s[20:23], s64 offen nt
	s_branch .Ladaln_end
.Ladaln_slow:
	v_bfe_u32 v1, v0, 2, 4
	v_mul_u32_u24 v1, 28, v1
	v_mad_u32_u24 v1, v3, 14, v1
	v_mad_u32_u24 v1, v5, s26, v1
	v_lshlrev_b32 v1, 1, v1
	s_cmp_eq_u32 s17, 0
	s_waitcnt lgkmcnt(0)
	s_load_dword s94, s[48:49], 0x0
	s_cselect_b64 s[20:21], s[36:37], s[38:39]
	s_cselect_b64 s[28:29], s[40:41], s[42:43]
	s_cbranch_scc1 .Ladaln_x2
	buffer_load_dwordx4 v[102:105], v1, s[28:31], 0 offen
	buffer_load_dwordx3 v[106:108], v1, s[28:31], 0 offen offset:16
	s_sleep )" STR(QD) R"(
	s_branch .Ladaln_go
.Ladaln_x2:
	s_cmpk_lt_u32 s2, 168
	s_cbranch_scc0 .Ladaln_idle
	s_waitcnt lgkmcnt(0)
	s_barrier
	s_barrier
	s_branch .Ladaln_go
.Ladaln_idle:
	s_endpgm
.Ladaln_st:
	s_and_b32 s18, s2, 15
	s_lshl_b32 s18, s18, 6
	s_lshr_b32 s19, s2, 4
	s_add_u32 s18, s18, s19
	s_lshl_b32 s18, s18, 12
	s_add_u32 s18, s18, s17
	s_lshl_b32 s18, s18, 4
	s_add_u32 s18, s18, 0x2ff400
	v_lshlrev_b32 v2, 4, v2
	v_lshl_add_u32 v2, v3, 13, v2
	s_cmpk_eq_u32 s17, 128
	s_waitcnt lgkmcnt(0)
	s_load_dword s94, s[48:49], 0x0
	s_cbranch_scc1 .Ladaln_end
	s_mov_b64 s[20:21], s[44:45]
	s_sleep )" STR(SD) R"(
	buffer_load_dwordx4 v[112:115], v2, s[20:23], s18 offen nt
	buffer_load_dwordx4 v[116:119], v2, s[20:23], s18 offen offset:128 nt
.Ladaln_end:
)"  : "={s[42:43]}"(xin), "={s[44:45]}"(kc), "={s[46:47]}"(vc), "={s[48:49]}"(ws), "+{s2}"(bid), "={s17}"(w64), "={s[84:91]}"(kb), "={s[92:93]}"(tz)
    : "{s[0:1]}"(__builtin_amdgcn_kernarg_segment_ptr()), "{v0}"(threadIdx.x)
    : "memory", "scc", "s18", "s19", "s20", "s21", "s22", "s23", "s26", "s28", "s29", "s30", "s31", "s36", "s37", "s38", "s39", "s40", "s41", "s50", "s51",
      "s52", "s53", "s54", "s55", "s56", "s57", "s58", "s59", "s60", "s61", "s62", "s63", "s64", "s94", "v1", "v2", "v3", "v5", "v102", "v167");
    v4u R[14], xs0;
    v3u xs1;
    v2u in;
    u32 tag;
    asm volatile("" : "={s94}"(tag));
    asm volatile("" : "={v0}"(tid), "={v4}"(io), "={v[110:111]}"(in), "={v[102:105]}"(xs0), "={v[106:108]}"(xs1),
                 "={v[112:115]}"(R[0]), "={v[116:119]}"(R[1]), "={v[120:123]}"(R[2]), "={v[124:127]}"(R[3]), "={v[128:131]}"(R[4]), "={v[132:135]}"(R[5]), "={v[136:139]}"(R[6]),
                 "={v[140:143]}"(R[7]), "={v[144:147]}"(R[8]), "={v[148:151]}"(R[9]), "={v[152:155]}"(R[10]), "={v[156:159]}"(R[11]), "={v[160:163]}"(R[12]), "={v[164:167]}"(R[13]));
    PIN();
    STAMP(1);
    const int w = w64 >> 6;
    const u32 l = tid & 63, idx = bid >> 3, xc = bid & 7;
    const u32 slab = bid & 15, split = bid >> 4;
    const u32 kq = l >> 4, a8 = l & 7, g8 = l >> 3;
    const bool isW = idx >= 24, isF = bid < 28;
#define rsW RS(ws, WS_END)
#define STAMP0()
#define ARGS() do { asm volatile("s_waitcnt lgkmcnt(0) ; args" : "+{s94}"(tag)); \
                    tag = tag * 1664525u + 1013904223u; STAMP0(); } while (0)
#define TILE2(n, i, r0, r1) PIN(); asm volatile("s_waitcnt vmcnt(" #n ") ; tile " r0 " " r1 : "+{v[" r0 "]}"(R[i]), "+{v[" r1 "]}"(R[i + 1]) :: "memory")

    if (__builtin_expect_with_probability(w < 3, 1, 0.55)) {
    if (__builtin_expect_with_probability(w < 2, 1, 0.9)) {
        v4u B0[8], B1[8], A[4];
        v4f c[4];
#pragma unroll
        for (int i = 0; i < 4; ++i) zero4(A[i]);
#pragma unroll
        for (int j = 0; j < 8; ++j) { B1[j][3] = 0; KEEP(B1[j]); }
        const u32 pl = OFF(l >= 32) + (2 * a8 + kq) * 16;
        if (__builtin_expect_with_probability(w == 0, 1, 0.55)) {
            if (bid >= 56) asm volatile("s_barrier" ::: "memory");
            u32 po = pl + WS_MP + (bid & 3) * 10752 + (bid >> 2) * 256 + (g8 & 1) * MPB;
            KEEP(po);
            asm volatile("s_waitcnt vmcnt(14) ; tile 102:111" : "+{v[110:111]}"(in) :: "memory");
            const u32 P0 = pk(silu(lo16(in.x)), silu(hi16(in.x))), P1 = pk(silu(lo16(in.y)), silu(hi16(in.y)));
            bank7<0>(A[0], A[2], P0, P1); bank7<1>(A[1], A[3], P0, P1);
            TILE2(12, 0, "112:115", "116:119"); tr2<0>(R[0], R[1], B0);
            TILE2(10, 2, "120:123", "124:127"); tr2<1>(R[2], R[3], B0);
            ARGS();
            TILE2(8, 4, "128:131", "132:135"); tr2<2>(R[4], R[5], B0);
            TILE2(6, 6, "136:139", "140:143"); tr2<3>(R[6], R[7], B0);
#pragma unroll
            for (int s = 0; s < 4; ++s) c[s] = mfma(A[0], B0[s], v4f{0, 0, 0, 0});
#pragma unroll
            for (int s = 0; s < 4; ++s) c[s] = mfma(A[1], B0[4 + s], c[s]);
            PIN();
            TILE2(4, 8, "144:147", "148:151"); tr2<0>(R[8], R[9], B1);
            TILE2(2, 10, "152:155", "156:159"); tr2<1>(R[10], R[11], B1);
            TILE2(0, 12, "160:163", "164:167");
            STAMP(2);
            tr2<2>(R[12], R[13], B1);
#pragma unroll
            for (int s = 0; s < 4; ++s) c[s] = mfma(A[2], B1[s], c[s]);
#pragma unroll
            for (int s = 0; s < 4; ++s) c[s] = mfma(A[3], B1[4 + s], c[s]);
            ST_C4(seal(v4f{fold(c[0]), fold(c[1]), fold(c[2]), fold(c[3])}, tag), rsW, po);
            STAMP(4);
            DUMP();
            EXIT();
        }
        asm volatile("s_waitcnt vmcnt(14) ; tile 102:111" : "+{v[110:111]}"(in), "+{v[102:105]}"(xs0), "+{v[106:108]}"(xs1) :: "memory");
        float h[4] = {lo16(in.x), hi16(in.x), lo16(in.y), hi16(in.y)};
        {
            float s1 = 0, s2 = 0;
#pragma unroll
            for (int i = 0; i < 7; ++i) {
                const u32 t = i < 4 ? xs0[i & 3] : xs1[i & 3];
                s1 += lo16(t) + hi16(t);
                s2 += lo16(t) * lo16(t) + hi16(t) * hi16(t);
            }
            s1 = sum_nb(s1); s2 = sum_nb(s2);
            float M, r;
            ln_stat(t32_add(s1, s1), t32_add(s2, s2), M, r);
#pragma unroll
            for (int i = 0; i < 4; ++i) h[i] = rb(rb(h[i] - M) * r);
        }
        asm volatile("" : "+v"(h[0]), "+v"(h[1]), "+v"(h[2]), "+v"(h[3]));
        ARGS();
        TILE2(12, 0, "112:115", "116:119"); tr2<0>(R[0], R[1], B0);
        TILE2(10, 2, "120:123", "124:127"); tr2<1>(R[2], R[3], B0);
        TILE2(8, 4, "128:131", "132:135"); tr2<2>(R[4], R[5], B0);
        TILE2(6, 6, "136:139", "140:143"); tr2<3>(R[6], R[7], B0);
        TILE2(4, 8, "144:147", "148:151"); tr2<0>(R[8], R[9], B1);
        TILE2(2, 10, "152:155", "156:159"); tr2<1>(R[10], R[11], B1);
        PIN();
        u32 po = pl + WS_QP + (bid >> 4) * 4096 + (bid & 3) * 1024 + (bid & 12) * 64 + (g8 & 1) * 65536;
        u32 mo = WS_MP + 2 * io + (l & 2) * ((MPB - 1792) / 2);
        asm volatile("" : "+v"(po), "+v"(mo));
        TILE2(0, 12, "160:163", "164:167"); tr2<2>(R[12], R[13], B1);
        KEEP8(B0); KEEP8(B1);
        v4u tr = tag_bytes(__builtin_amdgcn_alignbit(tag, tag, (io & 4) * 4));
        KEEP(tr);
        PIN();
        STAMP(2);
        until(tz, TM);
        v4u y[8];
        SPIN(SPIN_CAP) {
#pragma unroll
            for (int i = 0; i < 8; ++i) y[i] = LD_C4(rsW, mo + (i >> 2) * 1792, (i & 3) * 10752);
            FENCE();
            u32 bad = 0;
#pragma unroll
            for (int i = 0; i < 8; ++i) { y[i] ^= tr; bad |= or4(y[i]); }
            if (NONE(bad & 255u)) break;
        }
        STAMP(3);
        const v4f sa = (asf4(y[0]) + asf4(y[1])) + (asf4(y[2]) + asf4(y[3])), ca = (asf4(y[4]) + asf4(y[5])) + (asf4(y[6]) + asf4(y[7]));
#pragma unroll
        for (int i = 0; i < 4; ++i) h[i] = rb(h[i] * rb(1.f + rb(ca[i]))) + rb(sa[i]);
        const u32 P0 = pk(h[0], h[1]), P1 = pk(h[2], h[3]);
        bank7<0>(A[0], A[2], P0, P1); bank7<1>(A[1], A[3], P0, P1);
#pragma unroll
        for (int s = 0; s < 4; ++s) c[s] = mfma(A[0], B0[s], v4f{0, 0, 0, 0});
#pragma unroll
        for (int s = 0; s < 4; ++s) c[s] = mfma(A[2], B1[s], c[s]);
#pragma unroll
        for (int s = 0; s < 4; ++s) c[s] = mfma(A[1], B0[4 + s], c[s]);
#pragma unroll
        for (int s = 0; s < 4; ++s) c[s] = mfma(A[3], B1[4 + s], c[s]);
        ST_C4(seal(v4f{fold(c[0]), fold(c[1]), fold(c[2]), fold(c[3])}, tag), rsW, po);
        STAMP(4);
    } else {
        asm volatile("s_setprio 2 ; tile 102:166");
        ARGS();
    }
    const v4u tb = tag_bytes(tag);
    const u32 hd = slab * 2 + w - 1;
    until(tz, TP);
    {
        v4u y[4];
        const u32 qo = WS_QP + hd * 4096 + l * 16;
        SPIN(SPIN_CAP) {
#pragma unroll
            for (int i = 0; i < 4; ++i) y[i] = LD_C4(rsW, qo + i * 1024, 0);
            FENCE();
            u32 bad = 0;
#pragma unroll
            for (int i = 0; i < 4; ++i) { y[i] ^= tb; bad |= or4(y[i]); }
            if (NONE(bad & 255u)) break;
        }
        const v4f q = (asf4(y[0]) + asf4(y[1])) + (asf4(y[2]) + asf4(y[3]));
        *(v2u*)(lds + L_QS + (w - 1) * 640 + l * 8) = v2u{pk(q[0], q[1]), pk(q[2], q[3])};
    }
    STAMP(5);
    BAR();
    BAR();
    {
        const v2f ml = *(const v2f*)(lds + L_ML + (w - 1) * 64 + a8 * 8);
        const float M = max_lo3(ml.x);
        const float f = __builtin_amdgcn_exp2f((ml.x - M) * SC);
        const float L = sum_lo3(f * ml.y);
        ST_C4((v4u{asu(M), tag, asu(L), tag}), rsW, l ? OOB : WS_ST + (hd * 16 + split) * 128);
        float fs[8];
#pragma unroll
        for (int i = 0; i < 8; ++i) { fs[i] = rdl(f, i); KEEP(fs[i]); }
        u32 ra = L_OP + (w - 1) * 512 + (l >> 5) * 1024 + (l & 31) * 16, oa = WS_O + (hd * 16 + split) * 1024 + l * 16;
        asm volatile("" : "+v"(ra), "+v"(oa));
        STAMP(6);
        BAR();
        v4f x[8];
#pragma unroll
        for (int i = 0; i < 8; ++i) x[i] = *(const v4f*)(lds + ra + i * 2048);
        v4f o0 = x[0] * fs[0], o1 = x[1] * fs[1];
#pragma unroll
        for (int i = 2; i < 8; i += 2) { o0 += x[i] * fs[i]; o1 += x[i + 1] * fs[i + 1]; }
        ST_C4(seal(o0 + o1, tag), rsW, oa);
        STAMP(7);
    }
    const u32 ru = bid - 28;
    if (isW) asm volatile("s_barrier" ::: "memory");
    if (w == 2 || ru >= 28) { DUMP(); EXIT(); }
    const u32 rb_ = ru >= 14, cb = ru - rb_ * 14;
    const bool hi8 = l & 8;
    float m0, m1, m2;
    u32 e0, e1;
    u32 xo = rb_ * 896 + cb * 64 + a8 * 8 + kq * 2;
    {
        const u32 xv = __builtin_amdgcn_raw_buffer_load_b16(RS(xin, 1792), xo, 0, 0);
        const u32 mo = WS_MP + rb_ * MPB + 3584 + cb * 128 + (l & 8) * 224 + kq * 10752 + a8 * 16;
        v4u y0, y1;
        SPIN(SPIN_CAP) {
            y0 = LD_C4(rsW, mo, 0) ^ tb;
            y1 = LD_C4(rsW, mo + 3584, 0) ^ tb;
            FENCE();
            if (NONE((or4(y0) | or4(y1)) & 255u)) break;
        }
        const float t0 = rb(rows4(asf4(y0))), t1 = rb(rows4(asf4(y1))), o0 = dpp<ROR8>(t0), o1 = dpp<ROR8>(t1);
        m0 = lo16(xv); m1 = hi8 ? o0 : t0; m2 = hi8 ? t1 : o1;
        e0 = pk(1.f + t1, o0); e1 = dppu<QX1>(e0);
    }
    u32 oa = WS_WP + ru * 8192 + l * 16, ob = oa + 4096;
    u32 x1o = OFF(l & 9) + WS_X1 + (rb_ * 224 + cb * 16 + a8 * 2 + kq) * 16;
    ST_C4((v4u{e0, e1, e1, tag}), rsW, x1o + (WS_XE - WS_X1));
    xo = hi8 ? OOB : xo;
    asm volatile("" : "+v"(x1o), "+v"(xo), "+v"(m2), "+v"(oa), "+v"(ob));
    const rsrc_t rso = RS(out, 1792);
    { u32 tch = __builtin_amdgcn_raw_buffer_load_b16(rso, xo, 0, 16); KEEP(tch); }
    STAMP(8);
    v4f s;
    typedef u32 v32u __attribute__((ext_vector_type(32)));
    v32u z0, z1, z2, z3;
    const v4u rsw = {(u32)(u64)ws, (u32)((u64)ws >> 32) & 0xffffu, WS_END, 0x20000u};
    u32 cnt = SPIN_CAP;
    asm volatile(R"(
.macro r_issue b
	buffer_load_dwordx4 v[\b:\b+3], %6, %8, 0 offen sc1
	buffer_load_dwordx4 v[\b+4:\b+7], %6, %8, 0 offen offset:1024 sc1
	buffer_load_dwordx4 v[\b+8:\b+11], %6, %8, 0 offen offset:2048 sc1
	buffer_load_dwordx4 v[\b+12:\b+15], %6, %8, 0 offen offset:3072 sc1
	buffer_load_dwordx4 v[\b+16:\b+19], %7, %8, 0 offen sc1
	buffer_load_dwordx4 v[\b+20:\b+23], %7, %8, 0 offen offset:1024 sc1
	buffer_load_dwordx4 v[\b+24:\b+27], %7, %8, 0 offen offset:2048 sc1
	buffer_load_dwordx4 v[\b+28:\b+31], %7, %8, 0 offen offset:3072 sc1
.endm
.macro r_one b, n, first
	s_waitcnt vmcnt(\n)
	v_xor_b32 v[\b], %9, v[\b]
	v_xor_b32 v[\b+1], %10, v[\b+1]
	v_xor_b32 v[\b+2], %11, v[\b+2]
	v_xor_b32 v[\b+3], %12, v[\b+3]
.if \first == 1
	v_or_b32 v36, v[\b], v[\b+1]
.else
	v_or3_b32 v36, v[\b], v[\b+1], v36
.endif
.if \first == 2
	v_pk_add_f32 v[32:33], v[\b-4:\b-3], v[\b:\b+1]
	v_pk_add_f32 v[34:35], v[\b-2:\b-1], v[\b+2:\b+3]
.endif
.if \first == 0
	v_pk_add_f32 v[32:33], v[32:33], v[\b:\b+1]
	v_pk_add_f32 v[34:35], v[34:35], v[\b+2:\b+3]
.endif
	v_or3_b32 v36, v[\b+2], v[\b+3], v36
.endm
.macro r_check b
	r_one \b, 8*)" STR(RD) R"(-1, 1
	r_one \b+4, 8*)" STR(RD) R"(-2, 2
	r_one \b+8, 8*)" STR(RD) R"(-3, 0
	r_one \b+12, 8*)" STR(RD) R"(-4, 0
	r_one \b+16, 8*)" STR(RD) R"(-5, 0
	r_one \b+20, 8*)" STR(RD) R"(-6, 0
	r_one \b+24, 8*)" STR(RD) R"(-7, 0
	r_one \b+28, 8*)" STR(RD) R"(-8, 0
	v_cmp_ne_u32_sdwa vcc, v36, 0 src0_sel:BYTE_0 src1_sel:DWORD
	s_cbranch_vccz .Lr0_done
.endm
	.set r_d, 0
	.rept )" STR(RD) R"(-1
	r_issue 40+32*r_d
	s_sleep 4
	.set r_d, r_d+1
	.endr
.Lr0_loop:
	.set r_d, 0
	.rept )" STR(RD) R"(
	r_issue 40+32*((r_d+)" STR(RD) R"(-1)%%)" STR(RD) R"()
	r_check 40+32*r_d
	.set r_d, r_d+1
	.endr
	s_sub_u32 %5, %5, 1
	s_cbranch_scc0 .Lr0_loop
.Lr0_done:
)" : "=&{v[32:35]}"(s), "=&{v[40:71]}"(z0), "=&{v[72:103]}"(z1), "=&{v[104:135]}"(z2), "=&{v[136:167]}"(z3), "+s"(cnt)
   : "v"(oa), "v"(ob), "s"(rsw), "s"(tb.x), "s"(tb.y), "s"(tb.z), "s"(tb.w) : "memory", "vcc", "scc", "v36");
    STAMP(9);
    {
        float t = rows4(s);
        t += dpp<ROR8>(t);
        const u32 yb = pk(m0 + rb(m1 * rb(t)), 0.f);
        m0 = lo16(yb);
        float u = sum_lo3(hi8 ? m0 * m0 : m0);
        u = t16_add(u, u);
        u = t32_add(u, u);
        ST_C4((v4u{yb | (dppu<QX1>(yb) << 16), asu(u), dppu<ROR8>(asu(u)), tag}), rsW, x1o);
        __builtin_amdgcn_raw_buffer_store_b32(tag, rsW, (ru | l) ? OOB : 0, 0, 16);
    }
    STAMP(10);
    {
        oa += WS_FP - WS_WP; ob = OFF(l >= 32) + oa;
        v4u tc = l < 32 ? tb : v4u{0, 0, 0, 0};
        asm volatile("" : "+v"(oa), "+v"(ob), "+v"(tc));
        cnt = SPIN_CAP;
        asm volatile(R"(
.macro r2_issue b
	buffer_load_dwordx4 v[\b:\b+3], %6, %8, 0 offen sc1
	buffer_load_dwordx4 v[\b+4:\b+7], %6, %8, 0 offen offset:1024 sc1
	buffer_load_dwordx4 v[\b+8:\b+11], %6, %8, 0 offen offset:2048 sc1
	buffer_load_dwordx4 v[\b+12:\b+15], %7, %8, 0 offen offset:3072 sc1
.endm
.macro r2_one b, n, first, t0, t1, t2, t3
	s_waitcnt vmcnt(\n)
	v_xor_b32 v[\b], \t0, v[\b]
	v_xor_b32 v[\b+1], \t1, v[\b+1]
	v_xor_b32 v[\b+2], \t2, v[\b+2]
	v_xor_b32 v[\b+3], \t3, v[\b+3]
.if \first == 1
	v_or_b32 v36, v[\b], v[\b+1]
.else
	v_or3_b32 v36, v[\b], v[\b+1], v36
.endif
.if \first == 2
	v_pk_add_f32 v[32:33], v[\b-4:\b-3], v[\b:\b+1]
	v_pk_add_f32 v[34:35], v[\b-2:\b-1], v[\b+2:\b+3]
.endif
.if \first == 0
	v_pk_add_f32 v[32:33], v[32:33], v[\b:\b+1]
	v_pk_add_f32 v[34:35], v[34:35], v[\b+2:\b+3]
.endif
	v_or3_b32 v36, v[\b+2], v[\b+3], v36
.endm
.macro r2_check b
	r2_one \b, 4*)" STR(R2D) R"(-1, 1, %9, %10, %11, %12
	r2_one \b+4, 4*)" STR(R2D) R"(-2, 2, %9, %10, %11, %12
	r2_one \b+8, 4*)" STR(R2D) R"(-3, 0, %9, %10, %11, %12
	r2_one \b+12, 4*)" STR(R2D) R"(-4, 0, %13, %14, %15, %16
	v_cmp_ne_u32_sdwa vcc, v36, 0 src0_sel:BYTE_0 src1_sel:DWORD
	s_cbranch_vccz .Lr1_done
.if )" STR(R2P) R"(
	s_sleep )" STR(R2P) R"(
.endif
.endm
	.set r_d, 0
	.rept )" STR(R2D) R"(-1
	r2_issue 40+16*r_d
	s_sleep 3
	.set r_d, r_d+1
	.endr
.Lr1_loop:
	.set r_d, 0
	.rept )" STR(R2D) R"(
	r2_issue 40+16*((r_d+)" STR(R2D) R"(-1)%%)" STR(R2D) R"()
	r2_check 40+16*r_d
	.set r_d, r_d+1
	.endr
	s_sub_u32 %5, %5, 1
	s_cbranch_scc0 .Lr1_loop
.Lr1_done:
)" : "=&{v[32:35]}"(s), "+{v[40:71]}"(z0), "+{v[72:103]}"(z1), "+{v[104:135]}"(z2), "+{v[136:167]}"(z3), "+s"(cnt)
   : "v"(oa), "v"(ob), "s"(rsw), "s"(tb.x), "s"(tb.y), "s"(tb.z), "s"(tb.w), "v"(tc.x), "v"(tc.y), "v"(tc.z), "v"(tc.w) : "memory", "vcc", "scc", "v36");
    }
    STAMP(11);
    {
        float t = rows4(s);
        t += dpp<ROR8>(t);
        __builtin_amdgcn_raw_buffer_store_b16((u16)pk(m0 + rb(m2 * rb(t)), 0.f), rso, xo, 0, 16);
    }
    STAMP(12);
    asm volatile("s_waitcnt vmcnt(0)\n.Ladaln_e0:" :: "{v[40:71]}"(z0), "{v[72:103]}"(z1), "{v[104:135]}"(z2), "{v[136:167]}"(z3));
    DUMP();
    EXIT();
    }
    {
        asm volatile("; tile 102:111 120:163");
        const int v = w - 3;
        u32 kvb = ((slab << 6) + 48 + split) << 16;
        u32 ko = a8 * 8192 + g8 * 16 + v * 1024, vo = l * 16 + v * 1024;
        asm volatile("" : "+v"(ko), "+v"(vo));
        KEEPS(kvb);
        v4u K[8], V[8];
        const rsrc_t rsK = RS(kc + kvb, 65536), rsV = RS(vc + kvb, 65536);
#pragma unroll
        for (int i = 2; i < 8; ++i) {
            VMWAIT(win(i) - 1);
            K[i] = LD_NT(rsK, ko + i * 128, 0);
            FENCE();
        }
        asm volatile("; tile 102:167" : "+{v[112:115]}"(R[0]), "+{v[116:119]}"(R[1]), "+{v[164:167]}"(R[13]));
        K[0] = R[0]; K[1] = R[1];
        ARGS();
        const u32 wu = (idx & 7) * 8 + xc, hq = 2 * xc + ((idx >> 2) & 1), gq = idx & 3;
        const rsrc_t rsG = RS(isW ? wo + (size_t)(hq * 256 + (gq & 1) * 4 + (gq >> 1) * 2) * 896 + v * 128 : w1 + (size_t)v * 64 * 3584 + bid * 128,
                              v == 7 ? 0 : isW ? 249 * 896 + 128 : isF ? 63 * 3584 + 128 : 0);
        u32 gv0 = g8 * (isW ? 16 * 896 : 8 * 3584) + a8 * 16, gv1 = gv0 + (isW ? 128 * 896 : 4 * 3584);
        u32 gs1 = isW ? 896 : 3584, gs2 = isW ? 8 * 896 : 2 * 3584, gs3 = gs1 + gs2;
        u32 msel = ((l >> 4) ^ l) & 1 ? 4 : (l >> 1) & 3;
        u32 qa = L_QS + (l & 1) * 640 + (l & 2) * 8 + kq * 32, wa = L_OP + v * 2048 + l * 16;
        u32 ma = (l & 55) ? wa : L_ML + (l + v) * 8;
        asm volatile("" : "+v"(gv0), "+v"(gv1), "+v"(msel), "+v"(qa), "+v"(wa), "+v"(ma));
        asm volatile("" : "+s"(gs1), "+s"(gs2), "+s"(gs3));
#pragma unroll
        for (int i = 0; i < 8; ++i) {
            VMWAIT(win(8 + i) - 1);
            V[i] = LD_NT(rsV, vo, i * 8192);
            FENCE();
        }
        v4u G[8], PA[4], BV[8];
        ANY8(BV);
        tr2<0>(V[0], V[1], BV); tr2<1>(V[2], V[3], BV);
        KEEP8(BV);
#define GL(i) PIN(); G[i] = LD_NT(rsG, (i & 1) ? gv1 : gv0, i < 2 ? 0 : i < 4 ? gs1 : i < 6 ? gs2 : gs3); FENCE(); PIN()
        STAMP(2);
        BAR();
        STAMP(3);
        v4f s0 = {0, 0, 0, 0}, s1 = {0, 0, 0, 0};
        v4u Q[4];
#pragma unroll
        for (int j = 0; j < 4; ++j) Q[j] = *(const v4u*)(lds + qa + j * 128);
        GL(0);
#pragma unroll
        for (int j = 0; j < 4; ++j) {
            s0 = mfma(Q[j], K[j], s0);
            s1 = mfma(Q[j], K[4 + j], s1);
        }
        GL(1);
        const v4f sg = l < 32 ? s0 : s1;
        const float s = rb(fold(v4f{sg[0], sg[2], sg[1], sg[3]}));
        const float m = x32_max(max_lo3(s));
        GL(2);
        const float p = __builtin_amdgcn_exp2f((s - m) * SC);
        float ls = sum_lo3(p);
        ls = t32_add(ls, ls);
        *(v2f*)(lds + ma) = v2f{m, ls};
        GL(3);
        BAR();
        STAMP(4);
        const u32 pp = pk(p, dpp<QX1>(p));
        const u32 x0 = dppu<0x00>(pp), x1 = dppu<0xAA>(pp), y0 = dppu<HMIR>(x0), y1 = dppu<HMIR>(x1);
        GL(4);
#pragma unroll
        for (int c = 0; c < 4; ++c) PA[c] = msel == c ? (c < 2 ? v4u{x0, x1, y0, y1} : v4u{y0, y1, x0, x1}) : v4u{0, 0, 0, 0};
        asm volatile("" : "+v"(PA[0]), "+v"(PA[1]), "+v"(PA[2]), "+v"(PA[3]));
        GL(5);
        tr2<2>(V[4], V[5], BV);
        KEEP8(BV);
        GL(6);
        STAMPV(5, V[7]);
        tr2<3>(V[6], V[7], BV);
        {
            v4f o0 = {0, 0, 0, 0}, o1 = {0, 0, 0, 0};
#pragma unroll
            for (int c = 0; c < 4; ++c) {
                o0 = mfma(PA[c], BV[(c >> 1) * 4 + (c & 1)], o0);
                o1 = mfma(PA[c], BV[(c >> 1) * 4 + (c & 1) + 2], o1);
            }
            GL(7);
            *(v4f*)(lds + wa) = o0;
            *(v4f*)(lds + wa + 1024) = o1;
        }
        BAR();
        STAMP(6);
        if (!isW && !isF) {
            DUMP();
            EXIT();
        }
        v4u A[2];
        zero4(A[0]); zero4(A[1]);
        const v4u tb = tag_bytes(tag);
        u32 go = OFF(l >= 32) + (isW ? WS_WP + wu * 128 : WS_FP + bid * 128) + (((l >> 3) & 1) * 14 + 2 * v + (a8 >> 2)) * 8192 + ((2 * a8 + kq) & 7) * 16;
        u32 ra = L_P1 + (l & 2) * 192 + kq * 64 + (l & 1) * 32 + (l & 12) * 2;
        asm volatile("" : "+v"(go), "+v"(ra));
        u32 hv;
        if (isW) {
            const u32 sb = ((v >> 2) * 16 + hq) * 16, sp = (v & 3) * 4 + kq;
            u32 so = WS_ST + (sb + ((l + sp) & 15)) * 128;
            u32 oo = WS_O + (sb + sp) * 1024 + gq * 256 + (l & 15) * 16;
            asm volatile("" : "+v"(so), "+v"(oo));
            v4u st = LD_C4(rsW, so, 0), y = LD_C4(rsW, oo, 0);
            FENCE(); PIN();
            tr8(G, BV);
            u32 pa = L_P1 + (v & 3) * 2064 + (v >> 2) * 384 + (l & 15) * 16 + kq * 4;
            KEEP(pa);
            KEEP8(BV);
            PIN();
            SPIN(SPIN_CAP) {
                if (NONE((st.y ^ tag) | (st.w ^ tag))) break;
                st = LD_C4(rsW, so, 0); y = LD_C4(rsW, oo, 0);
                FENCE();
            }
            float f = __builtin_amdgcn_exp2f((asf(st.x) - max_row(asf(st.x))) * SC);
            f = dpp<0x150>(f) * __builtin_amdgcn_rcpf(sum_row(f * asf(st.z)));
            KEEP(f);
            PIN();
            STAMP(7);
            SPIN(SPIN_CAP) {
                y ^= tb;
                if (NONE(or4(y) & 255u)) break;
                y = LD_C4(rsW, oo, 0);
                FENCE();
            }
            STAMP(8);
            *(float*)(lds + pa) = rows4(asf4(y) * f);
            BAR();
            if (v == 7) { DUMP(); EXIT(); }
            const v2f t = (*(const v2f*)(lds + ra) + *(const v2f*)(lds + ra + 2064)) + (*(const v2f*)(lds + ra + 2 * 2064) + *(const v2f*)(lds + ra + 3 * 2064));
            hv = pk(t.x, t.y);
        } else {
            if (v == 7) { DUMP(); EXIT(); }
            const rsrc_t rs2 = RS(w2 + (size_t)bid * 57344 + v * 128, 63 * 896 + 128);
            const u32 v2 = g8 * 7168 + a8 * 16;
            __builtin_amdgcn_s_sleep(6);
#pragma unroll
            for (int i = 0; i < 8; ++i) V[i] = LD_NT(rs2, v2 + (i & 3) * 896, (i >> 2) * 3584);
            FENCE();
            v4u B1[8];
            tr8(G, B1);
            tr8(V, BV);
            const u32 si = (l & 16) / 2 + (l & 12) / 2 + (l & 1);
            const u32 ts_ = si < 14 ? tag : 0u;
            u32 xo = WS_X1 + ((l & 2) * 112 + v * 32 + kq * 8 + (l & 1) * 4 + ((l >> 2) & 3)) * 16;
            u32 so = OFF(si >= 14) + WS_X1 + ((l & 2) * 7 + si) * 256;
            u32 pa = L_P1 + v * 2064 + (l & 8) * 48 + (2 * a8 + (kq & 1)) * 16 + (l & 32) * 32;
            asm volatile("" : "+v"(xo), "+v"(so), "+v"(pa));
            KEEP8(B1);
            KEEP8(BV);
            PIN();
            v4u xg, sg2, xe;
            SPIN(SPIN_CAP) {
                xe = LD_C4(rsW, xo + (WS_XE - WS_X1), 0);
                FENCE();
                if (NONE(xe.w ^ tag)) break;
            }
            float c0 = lo16(xe.x), c1 = lo16(xe.y), b0 = hi16(xe.x), b1 = hi16(xe.y);
            asm volatile("" : "+v"(c0), "+v"(c1), "+v"(b0), "+v"(b1));
            STAMP(7);
            {
                v4u ys[FD], yx[FD];
#define F_ISSUE(d) ys[d] = LD_C4(rsW, so, 0); yx[d] = LD_C4(rsW, xo, 0)
#define F_CHECK(d) sg2 = ys[d]; xg = yx[d]; const u32 bad = (xg.w ^ tag) | (sg2.w ^ ts_)
                POLL(FD, FP_, F_ISSUE, F_CHECK);
            }
            STAMP(8);
            float M, r;
            ln_stat(sum_nb(asf(sg2.y)), sum_nb(asf(sg2.z)), M, r);
            float a0 = lo16(xg.x) - M, a1 = hi16(xg.x) - M;
            rb2(a0, a1);
            a0 *= r; a1 *= r;
            rb2(a0, a1);
            a0 *= c0; a1 *= c1;
            rb2(a0, a1);
            const v4f t1 = gemv(pk(a0 + b0, a1 + b1), A, B1);
            *(v4f*)(lds + pa) = t1;
            BAR();
            STAMP(9);
            v2f x[7];
#pragma unroll
            for (int i = 0; i < 7; ++i) x[i] = *(const v2f*)(lds + ra + i * 2064);
            const v2f t = ((x[0] + x[1]) + (x[2] + x[3])) + ((x[4] + x[5]) + x[6]);
            hv = pk(t.x, t.y);
            hv = pk(silu(lo16(hv)), silu(hi16(hv)));
        }
        ST_C4(seal(gemv(hv, A, BV), tag), rsW, go);
        STAMP(10);
        asm volatile(".Ladaln_e1:");
        DUMP();
        EXIT();
    }

}

extern "C" void kernel_launch(void* const* d_in, const int* in_sizes, int n_in, void* d_out, int out_size, void* d_ws, size_t ws_size,
                              hipStream_t stream) {
    adaln_block_kernel<<<256, 704, 0, stream>>>((const char*)d_in[9], (const char*)d_in[3], (const char*)d_in[8], (const char*)d_in[0],
                                                (const char*)d_in[1], (const char*)d_in[2], (char*)d_ws, (const char*)d_in[4],
                                                (const char*)d_in[6], (const char*)d_in[7], (char*)d_out);
}
```

```cpp
#define PROBE 5
#include <hip/hip_runtime.h>

typedef unsigned u32;
typedef unsigned long long u64;
typedef unsigned short u16;
typedef u32 v4u __attribute__((ext_vector_type(4)));
typedef u32 v2u __attribute__((ext_vector_type(2)));
typedef float v4f __attribute__((ext_vector_type(4)));
typedef float v2f __attribute__((ext_vector_type(2)));
typedef u32 v8u __attribute__((ext_vector_type(8)));
typedef u32 v16u __attribute__((ext_vector_type(16)));
typedef short v4s __attribute__((ext_vector_type(4)));
typedef __bf16 v8bf __attribute__((ext_vector_type(8)));
typedef __bf16 v2bf __attribute__((ext_vector_type(2)));
typedef __amdgpu_buffer_rsrc_t rsrc_t;

#define WIN 5
#define ESZ 0x14151515u
#define LSZ 0x12111111u
#define QSZ 0x0d0e0e0eu
#define QSLEEP 10
#define QPOS 6
#define W0 2
#define W1 3
#define PFWG 4
#define DRY 0

constexpr u32 WS_MOD = 4096;
constexpr u32 WS_Q = 32768;
constexpr u32 WS_ST = 65536;
constexpr u32 WS_X1 = 73728;
constexpr u32 WS_WP = 81920;
constexpr u32 WS_FP = 311296;
constexpr u32 WS_O = 524288;
constexpr u32 WS_END = 1048576;
constexpr u32 OOB = 0x40000000u;
#define SPIN_CAP_V 400000u
constexpr u32 SPIN_CAP = SPIN_CAP_V;

constexpr int L_FLG = 0;
constexpr int L_ML = 64;
constexpr int L_F = L_ML + 128;
constexpr int L_ST = L_F + 64;
constexpr int L_QS = L_ST + 256;
constexpr int L_PW = L_QS + 1024;
constexpr int L_RR = L_PW + 1792;
constexpr int L_P1 = L_RR + 1024;
constexpr int L_OP = L_P1 + 7168;
constexpr int L_END = L_OP + 14336;

#define RS(p, n) __builtin_amdgcn_make_buffer_rsrc((void*)(p), (short)0, (int)(n), 0x00020000)
#define LD_NT(rs, vo, so) __builtin_amdgcn_raw_buffer_load_b128(rs, (int)(vo), (int)(so), 2)
#define LD_C4(rs, vo) __builtin_amdgcn_raw_buffer_load_b128(rs, (int)(vo), 0, 16)
#define LD_C2(rs, vo) __builtin_amdgcn_raw_buffer_load_b64(rs, (int)(vo), 0, 16)
#define ST_C4(v, rs, vo) __builtin_amdgcn_raw_buffer_store_b128(v, rs, (int)(vo), 0, 16)
#define ST_C2(v, rs, vo) __builtin_amdgcn_raw_buffer_store_b64(v, rs, (int)(vo), 0, 16)
#define FENCE() asm volatile("" ::: "memory")
#define ARG(v, i) ((char*)(((u64)v[2 * (i) + 1] << 32) | v[2 * (i)]))
#define kc ARG(ka, 0)
#define wq ARG(ka, 1)
#define wmod ARG(ka, 2)
#define cin ARG(ka, 3)
#define xin ARG(ka, 4)
#define ws ARG(ka, 5)
#define vc ARG(ka, 6)
#define wo ARG(ka, 7)
#define w1 ARG(kb, 0)
#define w2 ARG(kb, 1)
#define out ARG(kb, 2)
#define EXIT() asm volatile("s_endpgm" ::: "memory")
#define KEEP(x) asm volatile("" : "+v"(x))
#define KEEPS(x) asm volatile("" : "+s"(x))
#define KEEP8(x) asm volatile("" : "+v"(x[0]), "+v"(x[1]), "+v"(x[2]), "+v"(x[3]), "+v"(x[4]), "+v"(x[5]), "+v"(x[6]), "+v"(x[7]))
#define ANY8(x) asm volatile("" : "=v"(x[0]), "=v"(x[1]), "=v"(x[2]), "=v"(x[3]), "=v"(x[4]), "=v"(x[5]), "=v"(x[6]), "=v"(x[7]))
__device__ __forceinline__ bool spin_more(u32& n) { asm volatile("" : "+s"(n)); return --n != 0; }
#define SPIN(n) _Pragma("unroll 1") for (u32 sp_ = (n), go_ = 1; go_; go_ = spin_more(sp_))
#define NONE(bad) (__builtin_amdgcn_ballot_w64((bad) != 0u) == 0ull)
#define PIN() __builtin_amdgcn_sched_barrier(0)
#define LDSWAIT() asm volatile("s_waitcnt lgkmcnt(0)" ::: "memory")
#define BAR() do { asm volatile("s_waitcnt lgkmcnt(0)\n\ts_barrier" ::: "memory"); } while (0)
#define WINWAIT() asm volatile("s_waitcnt vmcnt(%0)" :: "i"(WIN - 1) : "memory")

__device__ __forceinline__ float asf(u32 x) { return __builtin_bit_cast(float, x); }
__device__ __forceinline__ u32 asu(float x) { return __builtin_bit_cast(u32, x); }
__device__ __forceinline__ float lo16(u32 x) { return asf(x << 16); }
__device__ __forceinline__ float hi16(u32 x) { return asf(x & 0xffff0000u); }
__device__ __forceinline__ u32 pk(float a, float b) { v2bf v = {(__bf16)a, (__bf16)b}; return __builtin_bit_cast(u32, v); }
__device__ __forceinline__ float rb(float a) { return (float)(__bf16)a; }
__device__ __forceinline__ void rb2(float& a, float& b) { const u32 t = pk(a, b); a = lo16(t); b = hi16(t); }
template <int C> __device__ __forceinline__ float dpp(float x) { return asf((u32)__builtin_amdgcn_update_dpp(0, (int)asu(x), C, 0xf, 0xf, true)); }
template <int C> __device__ __forceinline__ u32 dppu(u32 x) { return (u32)__builtin_amdgcn_update_dpp(0, (int)x, C, 0xf, 0xf, true); }
constexpr int QX1 = 0xB1, QX2 = 0x4E, HMIR = 0x141, ROR1 = 0x121, ROR2 = 0x122, ROR4 = 0x124, ROR8 = 0x128;
__device__ __forceinline__ float t32_add(float a, float b) { auto s = __builtin_amdgcn_permlane32_swap(asu(a), asu(b), false, false); return asf(s[0]) + asf(s[1]); }
__device__ __forceinline__ float t16_add(float a, float b) { auto s = __builtin_amdgcn_permlane16_swap(asu(a), asu(b), false, false); return asf(s[0]) + asf(s[1]); }
__device__ __forceinline__ float x16_max(float x) { auto s = __builtin_amdgcn_permlane16_swap(asu(x), asu(x), false, false); return fmaxf(asf(s[0]), asf(s[1])); }
__device__ __forceinline__ float x32_max(float x) { auto s = __builtin_amdgcn_permlane32_swap(asu(x), asu(x), false, false); return fmaxf(asf(s[0]), asf(s[1])); }
__device__ __forceinline__ float rows4(float a0, float a1, float a2, float a3) { return t16_add(t32_add(a0, a2), t32_add(a1, a3)); }
__device__ __forceinline__ float sum_lo3(float x) { x += dpp<QX1>(x); x += dpp<QX2>(x); x += dpp<HMIR>(x); return x; }
__device__ __forceinline__ float max_lo3(float x) { x = fmaxf(x, dpp<QX1>(x)); x = fmaxf(x, dpp<QX2>(x)); x = fmaxf(x, dpp<HMIR>(x)); return x; }
__device__ __forceinline__ float sum_row(float x) { x = sum_lo3(x); return x + dpp<ROR8>(x); }
__device__ __forceinline__ float max_row(float x) { x = max_lo3(x); return fmaxf(x, dpp<ROR8>(x)); }
__device__ __forceinline__ v4f mfma(v4u a, v4u b, v4f c) {
    return __builtin_amdgcn_mfma_f32_16x16x32_bf16(__builtin_bit_cast(v8bf, a), __builtin_bit_cast(v8bf, b), c, 0, 0, 0);
}
__device__ __forceinline__ v4f mfma16(v2u a, v2u b, v4f c) {
    return __builtin_amdgcn_mfma_f32_16x16x16bf16_1k(__builtin_bit_cast(v4s, a), __builtin_bit_cast(v4s, b), c, 0, 0, 0);
}
__device__ __forceinline__ bool all_ok(bool ok) { return __builtin_amdgcn_ballot_w64(!ok) == 0ull; }
__device__ __forceinline__ float silu(float v) { return v * __builtin_amdgcn_rcpf(1.f + __builtin_amdgcn_exp2f(v * -1.44269504089f)); }
__device__ __forceinline__ float rdl(float v, int lane) { return asf((u32)__builtin_amdgcn_readlane((int)asu(v), lane)); }
constexpr float SC = 0.0625f * 1.44269504089f;

__device__ __forceinline__ void tr8(const v4u* r, v4u* o) {
#pragma unroll
    for (int j = 0; j < 8; ++j) {
        const u32 sel = (j & 1) ? 0x07060302u : 0x05040100u;
#pragma unroll
        for (int t = 0; t < 4; ++t) o[j][t] = __builtin_amdgcn_perm(r[2 * t + 1][j >> 1], r[2 * t][j >> 1], sel);
    }
}
__device__ __forceinline__ v4u seal(v4f a, u32 tag) {
    return v4u{__builtin_amdgcn_perm(asu(a[0]), tag, 0x07060500u), __builtin_amdgcn_perm(asu(a[1]), tag, 0x07060501u),
               __builtin_amdgcn_perm(asu(a[2]), tag, 0x07060502u), __builtin_amdgcn_perm(asu(a[3]), tag, 0x07060503u)};
}
__device__ __forceinline__ v4u tag_bytes(u32 t) { return v4u{t & 255u, (t >> 8) & 255u, (t >> 16) & 255u, t >> 24}; }
__device__ __forceinline__ v4u poll16(rsrc_t rs, u32 off, v4u tb) {
    v4u y;
    SPIN(SPIN_CAP) {
        y = LD_C4(rs, off) ^ tb;
        FENCE();
        if (NONE((y.x | y.y | y.z | y.w) & 255u)) break;
    }
    return y;
}

#define STAMP(i)
#define DUMP()

__device__ __forceinline__ void ln_part(char* lds, int w, int l, float a0, float a1) {
    const float K0 = asf((u32)__builtin_amdgcn_readfirstlane((int)asu(a0))), K1 = asf((u32)__builtin_amdgcn_readfirstlane((int)asu(a1)));
    const float d0 = a0 - K0, d1 = a1 - K1;
    float u = sum_row(rows4(d0, d1, d0 * d0, d1 * d1));
    const bool isk = (l & 47) == 1;
    u = isk ? ((l & 16) ? K1 : K0) : u;
    if ((l & 15) == 0 || isk) *(float*)(lds + L_ST + ((l >> 4) & 1) * 128 + w * 16 + (isk ? 8 : (l >> 5) * 4)) = u;
}
__device__ __forceinline__ void ln_fin(char* lds, int l, float& a0, float& a1) {
    const v4f s = *(const v4f*)(lds + L_ST + (l & 15) * 16);
    const bool live = (l & 7) != 7;
    const float M = sum_lo3(live ? s.x + 64.f * s.z : 0.f) * (1.f / 448);
    const float e = M - s.z;
    const float V = sum_lo3(live ? s.y + e * (64.f * e - 2.f * s.x) : 0.f) * (1.f / 448);
    const float r = __builtin_amdgcn_rcpf(rb(__builtin_amdgcn_sqrtf(rb(rb(V) + 1e-5f)))), Mb = rb(M);
    a0 -= rdl(Mb, 0); a1 -= rdl(Mb, 8);
    rb2(a0, a1);
    a0 *= rdl(r, 0); a1 *= rdl(r, 8);
    rb2(a0, a1);
}

template <int QD, int JJ> __device__ __forceinline__ u32 bank_mov(u32 old, u32 P) {
    constexpr int C = QD == JJ ? 0xE4 : QD > JJ ? 0x110 + 4 * (QD - JJ) : 0x100 + 4 * (JJ - QD);
    return (u32)__builtin_amdgcn_update_dpp((int)old, (int)P, C, 0xf, 1 << QD, false);
}
template <int QD> __device__ __forceinline__ void bank4(v4u& A, u32 P) {
    A[0] = bank_mov<QD, 0>(A[0], P); A[1] = bank_mov<QD, 1>(A[1], P); A[2] = bank_mov<QD, 2>(A[2], P); A[3] = bank_mov<QD, 3>(A[3], P);
}
__device__ __forceinline__ void build_a(u32 hv, u32 psel, v4u* A) {
    const u32 P = __builtin_amdgcn_perm(dppu<QX1>(hv), hv, psel);
    bank4<0>(A[0], P); bank4<1>(A[1], P); bank4<2>(A[2], P); bank4<3>(A[3], P);
}
__device__ __forceinline__ void zero_a(v4u* A) {
#pragma unroll
    for (int i = 0; i < 4; ++i) { A[i] = v4u{0, 0, 0, 0}; asm volatile("" : "+v"(A[i])); }
}
__device__ __forceinline__ v4f gemv(const v4u* A, const v4u* B) {
    v4f a0 = {0, 0, 0, 0}, a1 = {0, 0, 0, 0};
#pragma unroll
    for (int qd = 0; qd < 4; ++qd) {
        a0 = mfma(A[qd], B[2 * qd], a0);
        a1 = mfma(A[qd], B[2 * qd + 1], a1);
    }
    return v4f{a0[0] + dpp<ROR8>(a0[2]), a0[1] + dpp<ROR8>(a0[3]), a1[0] + dpp<ROR8>(a1[2]), a1[1] + dpp<ROR8>(a1[3])};
}
__device__ __forceinline__ u32 wvo(int l, u32 pitch) { return ((l >> 4) * 16 + ((l >> 3) & 1) * 2) * pitch + (l & 7) * 16; }
__device__ __forceinline__ v2f sum7(const char* p) {
    v2f s = *(const v2f*)p;
#pragma unroll
    for (int i = 1; i < 7; ++i) s += *(const v2f*)(p + i * 512);
    return s;
}

__global__ void __launch_bounds__(512, 2)
adaln_block_kernel(const char*, const char*, const char*, const char*, const char*, char*, const char*, const char*,
                   const char*, const char*, char*, char*) {
    __shared__ __attribute__((aligned(16))) char lds[L_END];
    STAMP(0);
    const u64 pt0 = __builtin_amdgcn_s_memrealtime();
    u64 pt1 = 0;
    v16u ka;
    v8u kb;
    u32 bid = blockIdx.x, tid = threadIdx.x;
    asm volatile("s_load_dwordx16 %0, %3, 0x0\n\ts_load_dwordx8 %1, %3, 0x40"
                 : "=&s"(ka), "=&s"(kb), "+s"(bid) : "s"(__builtin_amdgcn_kernarg_segment_ptr()));
    asm volatile("" : "+v"(tid));
    u32 pfv = 0;
    {
        u64 pc; u32 len;
        asm volatile("s_getpc_b64 %0\n.Ladaln_pc:\n\ts_max_i32 %1, .Ladaln_e0-.Ladaln_pc, 0\n\ts_max_i32 %1, .Ladaln_e1-.Ladaln_pc, %1\n\t"
                     "s_max_i32 %1, .Ladaln_e2-.Ladaln_pc, %1" : "=s"(pc), "=s"(len) :: "scc");
        const rsrc_t rsc = RS(pc, bid < PFWG * 8 ? len : 0);
        pfv = __builtin_amdgcn_raw_buffer_load_b32(rsc, tid * 128, 0, 0);
        asm volatile("" : "+s"(bid) :: "memory");
    }
    const u32 late4 = bid & 4, idx = bid >> 3, xc = bid & 7;
    const int rel = idx + late4 - 4;
    const bool isX = rel < 0, isQ = rel < 8;
    u32 ng = (u32)((((u64)LSZ << 32) | ESZ) >> (xc * 8)) & 255u;
    u32 g0 = ((idx >> 2) + (late4 >> 1) - 3) * ng;
    g0 = late4 ? 256 - g0 : g0;
    u32 slab = ((bid >> 1) & 12) | (bid & 3);
    u32 qn = xc * 8 + idx;
    KEEPS(qn);
    qn += late4 - 4;
    u32 split = (late4 ? 3 : -2) + (rel >> 2);
    {
        const u32 sh = (xc & 3) * 8, e5 = ((ESZ >> sh) & 255) * 5, qe = (QSZ >> sh) & 255, rem = 256 - e5 - ((LSZ >> sh) & 255) * 6 - 2 * qe, hi = (rel >> 2) & 1;
        const u32 qg = e5 + (late4 ? 2 * qe + (hi ? rem >> 1 : 0) : hi * qe), qk = late4 ? (hi ? rem - (rem >> 1) : rem >> 1) : qe;
        g0 = isQ ? qg : g0;
        ng = isQ ? qk : ng;
        split = isQ ? 11 + (late4 >> 1) + hi : split;
    }
    u32 kvb = ((slab << 10) + 0x300 + g0) << 12, kvn = ng << 12;
    int fu = (rel - 8) * 8 + xc;
    const bool isF = !isQ && fu < 28;
    const int w = __builtin_amdgcn_readfirstlane(tid >> 6);
    const int l = tid & 63, col = tid;
    u32 psel = (l & 1) ? 0x03020706u : 0x05040100u;
    const int gpos = ((l & 7) * 8 + (l >> 4) * 2) * 8;
    u32 kvo = (tid & 7) * 512 + (tid & 8) * 8 + (tid & 48);
    u32 oa = WS_O + ((slab * 2 + (w & 1)) * 16 + split) * 1024 + l * 16;
    u32 ra = L_OP + (w & 1) * 1024 + l * 8;
    const u32 pitch = isX ? 5376u : 8192u;
    u32 n0 = isQ ? pitch * 448u : kvn, nx = isQ ? 1792u : 0u, ko = isQ ? (isX ? slab : qn) << 7 : kvb;
    u32 s1 = isQ ? pitch : 128u, s2 = isQ ? pitch * 4 : 256u, s3 = s1 + s2;
    u32 vo = isQ ? ((tid & 0x1f0) | ((tid >> 2) & 2)) * pitch + (tid & 7) * 16 : (tid >> 6) * 4096 + kvo;
    u32 vo2 = vo + (isQ ? pitch * 8 : 28672u), xo2 = tid * 2;
    asm volatile("" : "+v"(vo), "+v"(vo2), "+v"(xo2), "+v"(kvo), "+v"(oa), "+v"(ra), "+v"(psel));
    asm volatile("" : "+s"(kvb), "+s"(kvn), "+s"(ng), "+s"(split), "+s"(slab), "+s"(qn), "+s"(fu));
    STAMP(3);
    asm volatile("s_waitcnt lgkmcnt(0)" : "+s"(ka), "+s"(kb), "+s"(n0), "+s"(nx), "+s"(ko), "+s"(s1), "+s"(s2), "+s"(s3));
    STAMP(4);
    u32 tag = *(const __attribute__((address_space(4))) u32*)(u64)ws;
#define GET_TAG() do { KEEPS(tag); tag = tag * 1664525u + 1013904223u; } while (0)
    const rsrc_t rsW = RS(ws, WS_END);
    v4u G[8];

    if (__builtin_expect(w < 7, 1)) {
        v4u R[8];
        u32 xi0, xi1;
        {
            const rsrc_t rsv = RS(isX ? cin : xin, nx);
            xi0 = __builtin_amdgcn_raw_buffer_load_b16(rsv, xo2, 0, 0);
            xi1 = __builtin_amdgcn_raw_buffer_load_b16(rsv, xo2 + 896, 0, 0);
            const char* fp = isX ? wmod : wq;
            const rsrc_t rs0 = RS((isQ ? fp : kc) + ko, n0);
#define LD0(i) R[i] = LD_NT(rs0, (i) < 4 ? vo : vo2, ((i) & 3) == 0 ? 0u : ((i) & 3) == 1 ? s1 : ((i) & 3) == 2 ? s2 : s3)
#pragma unroll
            for (int i = 0; i < W0; ++i) LD0(i);
            FENCE();
            if (!isQ) asm volatile("s_waitcnt vmcnt(%0)" :: "i"(W0 - 1) : "memory");
#pragma unroll
            for (int i = W0; i < W0 + W1; ++i) LD0(i);
            FENCE();
            if (!isQ) asm volatile("s_waitcnt vmcnt(%0)" :: "i"(W1) : "memory");
#pragma unroll
            for (int i = W0 + W1; i < 8; ++i) LD0(i);
            FENCE();
            PIN();
        }
        if (isX) {
            if (slab >= 14) EXIT();
            const rsrc_t rs0 = RS(wmod, 448 * 5376);
            u32 so = slab * 128;
            v4u B[8], A[4];
            zero_a(A);
            build_a(pk(silu(lo16(xi0)), silu(lo16(xi1))), psel, A);
#pragma unroll 1
            for (int u = 0; u < 3; ++u) {
                tr8(R, B);
                KEEP8(B);
                if (u == 0) STAMP(2);
                const v4f t = gemv(A, B);
                if (!(l & 8)) *(v4f*)(lds + L_P1 + (u & 1) * 3584 + w * 512 + gpos) = t;
                BAR();
                so = u == 2 ? OOB : so + 14 * 128;
#pragma unroll
                for (int i = 0; i < 8; ++i) R[i] = LD_NT(rs0, vo, so + ((i >> 1) * 4 + (i & 1)) * 5376);
                FENCE();
            }
            asm volatile(".Ladaln_e2:");
            DUMP();
            EXIT();
        }
        GET_TAG();
        v4u K2[4], V[12];
        const rsrc_t rsK = RS(kc + kvb, kvn), rsV = RS(vc + kvb, kvn);
        u32 sl[3] = {w * 4096u, w * 4096u + 28672u, w * 4096u + 57344u};
        asm volatile("" : "+s"(sl[0]), "+s"(sl[1]), "+s"(sl[2]));
#define LDK(i) LD_NT(rsK, kvo + ((i) & 3) * 128, sl[(i) >> 2])
#define LDV(i) V[i] = LD_NT(rsV, l * 16 + ((i) & 3) * 1024, sl[(i) >> 2])
#define LDG(i) G[i] = LD_NT(rsG, ((i) & 1) ? gv1 : gv0, gs[(i) >> 1])
        char* pw = lds + L_PW + w * 256;
        if (l < 32) *(u32*)(pw + 128 + l * 4) = 0;
        float a0 = lo16(xi0), a1 = lo16(xi1);
        if (isQ) ln_part(lds, w, l, a0, a1);
        BAR();
        if (isQ) {
            ln_fin(lds, l, a0, a1);
            KEEP(a0); KEEP(a1);
            v4u B[8], A[4];
            zero_a(A);
            tr8(R, B);
            KEEP8(B);
            PIN();
            STAMP(1);
            v2u sa, ca;
            SPIN(SPIN_CAP) {
                sa = LD_C2(rsW, WS_MOD + col * 8);
                ca = LD_C2(rsW, WS_MOD + 3584 + col * 8);
                FENCE();
                if (NONE((sa.y ^ tag) | (ca.y ^ tag))) break;
            }
            STAMP(2);
            build_a(pk(rb(a0 * rb(1.f + lo16(ca.x))) + lo16(sa.x), rb(a1 * rb(1.f + hi16(ca.x))) + hi16(sa.x)), psel, A);
            const v4f t = gemv(A, B);
            if (!(l & 8)) *(v4f*)(lds + L_P1 + w * 512 + gpos) = t;
            BAR();
#pragma unroll
            for (int i = 0; i < 8; ++i) R[i] = LDK(i);
            FENCE(); PIN();
        }
#pragma unroll
        for (int i = 0; i < 4; ++i) { WINWAIT(); K2[i] = LDK(8 + i); }
        const int row = l & 15, kq = l >> 4;
        const rsrc_t rsG = RS(isQ ? wo + (size_t)((qn >> 2) * 256 + (qn & 3) * 2) * 896 : w2 + (size_t)(isF ? fu : 0) * 57344, isQ ? 250 * 896 : isF ? 57344 : 0);
        u32 gv0 = (isQ ? kq * 32 + (row >> 3) : kq * 16 + (row >> 3) * 2) * 896 + (l & 7) * 16, gv1 = gv0 + (isQ ? 128 * 896 : 896);
        const u32 gst = isQ ? 7168u : 3584u;
        u32 gs[4] = {w * 128u, w * 128u + gst, w * 128u + 2 * gst, w * 128u + 3 * gst};
        u32 wa = L_OP + w * 2048 + l * 8;
        asm volatile("" : "+v"(gv0), "+v"(gv1), "+v"(wa));
        asm volatile("" : "+s"(gs[0]), "+s"(gs[1]), "+s"(gs[2]), "+s"(gs[3]));
        float s = 0;
        v4u PA[4];
        v2u PB[4];
#pragma unroll
        for (int i = 0; i < 12; ++i) {
            WINWAIT(); LDV(i);
            FENCE(); PIN();
            if (i == QPOS) {
                for (;;) {
                    FENCE();
                    if (__builtin_amdgcn_readfirstlane(*(const u32*)(lds + L_FLG))) break;
                    __builtin_amdgcn_s_sleep(1);
                }
                FENCE();
                STAMP(6);
                const char* qb = lds + L_QS + (row & 1) * 512 + ((row >> 1) & 1) * 64 + kq * 16;
                v4f sc[3];
#pragma unroll
                for (int g = 0; g < 3; ++g) sc[g] = v4f{0, 0, 0, 0};
#pragma unroll
                for (int j = 0; j < 4; ++j) {
                    const v4u A = *(const v4u*)(qb + j * 128);
#pragma unroll
                    for (int g = 0; g < 3; ++g) sc[g] = mfma(A, g < 2 ? R[g * 4 + j] : K2[j], sc[g]);
                }
                const v4f sg = kq == 0 ? sc[0] : kq == 1 ? sc[1] : sc[2];
                s = (row < 8 ? sg[0] : sg[3]) + dpp<ROR8>(row < 8 ? sg[1] : sg[2]);
                s = (kq < 3 && kq * 7 + w < (int)ng) ? rb(s) : -INFINITY;
                PIN();
            }
            if (i == QPOS + 1) {
                const float m = x32_max(x16_max(max_lo3(s)));
                const float p = __builtin_amdgcn_exp2f((s - (m == -INFINITY ? 0.f : m)) * SC);
                *(u16*)(pw + (kq >> 1) * 64 + (l & 1) * 32 + ((l >> 3) & 1) * 16 + ((kq & 1) * 4 + ((l >> 1) & 3)) * 2) = (u16)pk(p, p);
                float ls = sum_lo3(p);
                ls = t16_add(ls, ls);
                ls = t32_add(ls, ls);
                if ((l & 55) == 0) *(v2f*)(lds + L_ML + (l + w) * 8) = v2f{m, ls};
                PIN();
            }
            if (i == QPOS + 2) {
                LDSWAIT();
                const int real = (kq >> 1) * 32 + ((row >> 1) & 1) * 16;
                const bool on = (kq & 1) == (row & 1);
#pragma unroll
                for (int qd = 0; qd < 4; ++qd) {
                    const char* ap = pw + ((on && (row >> 2) == qd) ? real : 128);
                    PA[qd] = *(const v4u*)ap;
                    PB[qd] = *(const v2u*)(ap + 64);
                }
                PIN();
            }
        }
        STAMP(5);
        WINWAIT(); LDG(0);
        FENCE(); PIN();
        v4f o0 = {0, 0, 0, 0}, o1 = {0, 0, 0, 0};
        {
            v4u BV[8];
            tr8(V, BV);
#pragma unroll
            for (int qd = 0; qd < 4; ++qd) {
                o0 = mfma(PA[qd], BV[2 * qd], o0);
                o1 = mfma(PA[qd], BV[2 * qd + 1], o1);
            }
        }
        KEEP(o0); KEEP(o1);
        PIN();
        STAMP(7);
#pragma unroll
        for (int i = 1; i < 3; ++i) { WINWAIT(); LDG(i); }
        FENCE(); PIN();
        {
            v2u BV[8];
#pragma unroll
            for (int j = 0; j < 8; ++j) BV[j].x = __builtin_amdgcn_perm(V[9][j >> 1], V[8][j >> 1], (j & 1) ? 0x07060302u : 0x05040100u);
            PIN();
            WINWAIT();
#pragma unroll
            for (int i = 3; i < 8; ++i) LDG(i);
            FENCE(); PIN();
#pragma unroll
            for (int j = 0; j < 8; ++j) BV[j].y = __builtin_amdgcn_perm(V[11][j >> 1], V[10][j >> 1], (j & 1) ? 0x07060302u : 0x05040100u);
#pragma unroll
            for (int qd = 0; qd < 4; ++qd) {
                o0 = mfma16(PB[qd], BV[2 * qd], o0);
                o1 = mfma16(PB[qd], BV[2 * qd + 1], o1);
            }
        }
        *(v2f*)(lds + wa) = v2f{o0[0], o0[1]};
        *(v2f*)(lds + wa + 1024) = v2f{o0[2], o0[3]};
        *(v2f*)(lds + wa + 512) = v2f{o1[0], o1[1]};
        *(v2f*)(lds + wa + 1536) = v2f{o1[2], o1[3]};
        FENCE();
    } else {
        GET_TAG();
        __builtin_amdgcn_raw_buffer_store_b32((u32)pt0, rsW, l == 0 ? 1024 + blockIdx.x * 4 : OOB, 0, 16);
        if (isX) {
            if (slab >= 14) EXIT();
#pragma unroll 1
            for (int u = DRY ? -1 : 0; u < 3; ++u) {
                if (u >= 0) BAR();
                const v2f s = sum7(lds + L_P1 + (u & 1) * 3584 + l * 8);
                ST_C2((v2u{pk(s.x, s.y), tag}), rsW, u < 0 ? OOB : WS_MOD + ((slab + u * 14) * 64 + l) * 8);
                if (u == 0) STAMP(3); else STAMP(4);
            }
            DUMP();
            EXIT();
        }
        if (l < 16) *(v2u*)(lds + L_ML + l * 8) = (l & 7) == 7 ? v2u{0xff800000u, 0u} : v2u{~0u, ~0u};
        if (l == 0) *(u32*)(lds + L_FLG) = 0;
        BAR();
        if (isQ) {
            BAR();
            const v2f s = sum7(lds + L_P1 + l * 8);
            const u32 hv = pk(s.x, s.y);
            ST_C2((v2u{__builtin_amdgcn_perm(dppu<QX1>(hv), hv, psel), tag}), rsW, WS_Q + (l & 1) * 16384 + (qn * 32 + (l >> 1)) * 8);
            STAMP(3);
        } else __builtin_amdgcn_s_sleep(QSLEEP);
        v4u qa, qb;
        const u32 qo = WS_Q + slab * 2048 + l * 16;
        SPIN(SPIN_CAP) {
            qa = LD_C4(rsW, qo);
            qb = LD_C4(rsW, qo + 1024);
            FENCE();
            if (NONE((qa.y ^ tag) | (qa.w ^ tag) | (qb.y ^ tag) | (qb.w ^ tag))) break;
        }
        *(v2u*)(lds + L_QS + l * 8) = v2u{qa.x, qa.z};
        *(v2u*)(lds + L_QS + 512 + l * 8) = v2u{qb.x, qb.z};
        LDSWAIT();
        *(u32*)(lds + L_FLG) = 1;
        pt1 = __builtin_amdgcn_s_memrealtime();
        FENCE();
        STAMP(5);
        v2u ml;
        SPIN(SPIN_CAP * 16) {
            FENCE();
            ml = *(const v2u*)(lds + L_ML + (l & 15) * 8);
            if (all_ok(ml.x != ~0u && ml.y != ~0u)) break;
            __builtin_amdgcn_s_sleep(1);
        }
        const float M = max_lo3(asf(ml.x));
        const float f = __builtin_amdgcn_exp2f((asf(ml.x) - M) * SC);
        const float L = sum_lo3(f * asf(ml.y));
        if (l < 16) *(float*)(lds + L_F + l * 4) = f;
        if ((l & 55) == 0) ST_C4((v4u{asu(M), tag, asu(L), tag}), rsW, WS_ST + ((slab * 2 + (l >> 3)) * 16 + split) * 16);
        STAMP(7);
        asm volatile(".Ladaln_e1:");
        ANY8(G);
    }
#pragma unroll 1
    for (int it = (DRY && w == 7) ? 0 : 1; it < 2; ++it) {
        if (it) BAR();
        if (w >= 6) {
            const char* fp = lds + L_F + (w & 1) * 32;
            v4f fa = *(const v4f*)fp, fb = *(const v4f*)(fp + 16);
            v2f x[7], y[7];
#pragma unroll
            for (int i = 0; i < 7; ++i) { x[i] = *(const v2f*)(lds + ra + i * 2048); y[i] = *(const v2f*)(lds + ra + i * 2048 + 512); }
            PIN();
            v2f ax = x[0] * fa[0], ay = y[0] * fa[0];
#pragma unroll
            for (int i = 1; i < 7; ++i) { const float f = i < 4 ? fa[i] : fb[i - 4]; ax += x[i] * f; ay += y[i] * f; }
            ST_C4(seal(v4f{ax.x, ax.y, ay.x, ay.y}, tag), rsW, it ? oa : OOB);
        }
    }
    STAMP(9);
    if (!isQ && !isF) { DUMP(); EXIT(); }

    v4u BG[8], B[8], A[4], R[8];
    zero_a(A);
    ANY8(BG); ANY8(B); ANY8(R);
    u32 rel4 = idx + late4;
    KEEPS(rel4);
    const bool onlyQ = rel4 < 12;
    const int hq = qn >> 2, sp4 = (w >> 1) * 4 + (l >> 4), bb = (l >> 3) & 1, c7 = fu * 16 + ((l & 31) >> 1);
    const u32 po = ((l >> 3) * 8 + w) * 3584 + fu * 128 + (l & 7) * 16;
    const u32 oo = sp4 == 15 ? OOB : WS_O + ((bb * 16 + hq) * 16 + sp4) * 1024 + (qn & 3) * 256 + (w & 1) * 128 + (l & 7) * 16;
    u32 off = isQ ? oo : WS_WP + po;
    v4u tb = tag_bytes((isQ && sp4 == 15) ? 0u : tag);
    char* wa = lds + (isQ ? L_P1 + (w >> 1) * 512 + ((w & 1) * 32 + (l & 7) * 4 + (l >> 4)) * 8 + bb * 4 : L_RR + ((l & 7) * 4 + (l >> 4)) * 32 + w * 4);
    const u32 go = (l & 8) ? OOB : (isQ ? WS_WP + qn * 3584 : WS_FP + fu * 3584) + w * 512 + gpos;
    const u32 mo0 = WS_MOD + (w < 7 ? 1344 + col : 896 + c7) * 8, mo1 = WS_MOD + (w < 7 ? 1792 + col : 2240 + c7) * 8;
    const u32 xo = (l & 1) * 896 + c7 * 2;
    const rsrc_t rso = RS(out, 1792);
    v2u ma = {0, 0}, mb = {0, 0};
    u32 xv = 0;
    if (isF) {
        ma = LD_C2(rsW, mo0);
        mb = LD_C2(rsW, mo1);
        xv = __builtin_amdgcn_raw_buffer_load_b16(RS(xin, 1792), xo, 0, 0);
        const rsrc_t rs1 = RS(w1, 448 * 3584);
        const u32 v1 = wvo(l, 3584);
#pragma unroll
        for (int i = 0; i < 8; ++i) R[i] = LD_NT(rs1, v1, w * 64 * 3584 + fu * 128 + ((i >> 1) * 4 + (i & 1)) * 3584);
        FENCE();
    }
    if (onlyQ && w < 3) *(v2f*)(lds + L_P1 + (4 + w) * 512 + l * 8) = v2f{0.f, 0.f};
    float fac = 1.f;
    float m0 = 0, m1 = 0, m2 = 0, m3 = 0;
#pragma unroll 1
    for (int ph = DRY ? 0 : 1; ph < 3; ++ph) {
        v4u y = {0, 0, 0, 0};
        if (ph == 1) {
            if (onlyQ) {
                const bool dead = (l & 15) == 15;
                const u32 tl = dead ? 0u : tag;
                v4u st;
                SPIN(SPIN_CAP) {
                    st = LD_C4(rsW, dead ? OOB : WS_ST + (((l & 16) + hq) * 16 + (l & 15)) * 16);
                    FENCE();
                    if (NONE((st.y ^ tl) | (st.w ^ tl))) break;
                }
                const float m = dead ? -INFINITY : asf(st.x);
                float f = __builtin_amdgcn_exp2f((m - max_row(m)) * SC);
                f *= __builtin_amdgcn_rcpf(sum_row(f * asf(st.z)));
                fac = asf((u32)__builtin_amdgcn_ds_bpermute((bb * 16 + sp4) * 4, (int)asu(f)));
            }
            if (isF) {
                SPIN(SPIN_CAP) {
                    if (NONE((ma.y ^ tag) | (mb.y ^ tag))) break;
                    ma = LD_C2(rsW, mo0);
                    mb = LD_C2(rsW, mo1);
                    FENCE();
                }
                asm volatile("" : "+v"(xv));
                const float ga = (l & 1) ? hi16(ma.x) : lo16(ma.x), gb = (l & 1) ? hi16(mb.x) : lo16(mb.x), c1 = rb(1.f + hi16(mb.x));
                m0 = lo16(w < 7 ? ma.x : xv); m1 = w < 7 ? hi16(ma.x) : ga; m2 = rb(1.f + lo16(mb.x)); m3 = w < 7 ? c1 : gb;
                KEEP8(R);
                tr8(R, B);
            }
            KEEP8(G);
            tr8(G, BG);
            KEEP(fac);
            PIN();
            STAMP(10);
        }
        if (ph) {
            y = poll16(rsW, off, tb);
            if (ph == 1) STAMP(11); else STAMP(14);
        }
        float t = rows4(asf(y.x) * fac, asf(y.y) * fac, asf(y.z) * fac, asf(y.w) * fac);
        const float t2 = t + dpp<ROR8>(t);
        *(float*)wa = isQ ? t : t2;
        BAR();
        if (w == 7 && isF) {
            const v4f q = *(const v4f*)(lds + L_RR + (l & 31) * 32 + (l >> 5) * 16);
            float s = (q[0] + q[1]) + (q[2] + q[3]);
            s = t32_add(s, s);
            const u32 yb = pk(m0 + rb(m1 * rb(s)), 0.f);
            ST_C2((v2u{yb | (dppu<QX1>(yb) << 16), tag}), rsW, (ph == 1 && !(l & 33)) ? WS_X1 + c7 * 8 : OOB);
            __builtin_amdgcn_raw_buffer_store_b16((u16)yb, rso, (ph == 2 && l < 32) ? xo : OOB, 0, 0);
            __builtin_amdgcn_raw_buffer_store_b32(tag, rsW, (ph == 2 && fu == 0 && l == 0) ? 0 : OOB, 0, 16);
            m0 = lo16(yb); m1 = m3;
            if (ph == 1) STAMP(12);
        }
        if (ph == 2 && fu == 0 && w == 7) {
            const u64 now = __builtin_amdgcn_s_memrealtime();
            const v4u sv = LD_C4(rsW, 1024 + l * 16);
            int a = (int)(sv.x - (u32)pt0), b = (int)(sv.y - (u32)pt0), c = (int)(sv.z - (u32)pt0), d = (int)(sv.w - (u32)pt0);
            int mx = max(max(a, b), max(c, d)), mn = min(min(a, b), min(c, d)), sm = a + b + c + d;
            for (int o = 1; o < 64; o <<= 1) { mx = max(mx, __shfl_xor(mx, o)); mn = min(mn, __shfl_xor(mn, o)); sm += __shfl_xor(sm, o); }
            int q = PROBE == 0 ? 0 : PROBE == 1 ? (int)(now - pt0) : PROBE == 2 ? mx : PROBE == 3 ? -mn : PROBE == 4 ? (sm >> 8) - mn : (int)(pt1 - pt0);
            q = __builtin_amdgcn_readfirstlane(q);
            q = q < 0 ? 0 : q > 5000 ? 5000 : q;
            const u64 tgt = pt0 + 6000 + 4 * q;
            while (__builtin_amdgcn_s_memrealtime() < tgt) __builtin_amdgcn_s_sleep(1);
        }
        if (ph == 2) { STAMP(15); DUMP(); EXIT(); }
        if (isF) {
            float a0, a1;
            if (w < 7) {
                v2u xg = {0, 0};
                if (ph) {
                    SPIN(SPIN_CAP) {
                        xg = LD_C2(rsW, WS_X1 + col * 8);
                        FENCE();
                        if (NONE(xg.y ^ tag)) break;
                    }
                    STAMP(12);
                }
                a0 = lo16(xg.x); a1 = hi16(xg.x);
                ln_part(lds, w, l, a0, a1);
            }
            BAR();
            if (w < 7) {
                ln_fin(lds, l, a0, a1);
                a0 *= m2; a1 *= m3;
                rb2(a0, a1);
                build_a(pk(a0 + m0, a1 + m1), psel, A);
                const v4f t1 = gemv(A, B);
                if (!(l & 8)) *(v4f*)(lds + L_P1 + w * 512 + gpos) = t1;
            }
            BAR();
        }
        if (w < 7) {
            const v2f s = sum7(lds + L_P1 + l * 8);
            u32 hv = pk(s.x, s.y);
            if (isF) hv = pk(silu(lo16(hv)), silu(hi16(hv)));
            build_a(hv, psel, A);
            ST_C4(seal(gemv(A, BG), tag), rsW, ph ? go : OOB);
            if (ph) STAMP(13);
        }
        if (ph) {
            if (isQ) { DUMP(); EXIT(); }
            const bool dead = (l >> 3) * 8 + w >= 28;
            off = dead ? OOB : WS_FP + po;
            tb = tag_bytes(dead ? 0u : tag);
        }
    }
    asm volatile(".Ladaln_e0:" :: "v"(pfv));
}

extern "C" void kernel_launch(void* const* d_in, const int* in_sizes, int n_in, void* d_out, int out_size, void* d_ws, size_t ws_size,
                              hipStream_t stream) {
    adaln_block_kernel<<<256, 512, 0, stream>>>((const char*)d_in[1], (const char*)d_in[3], (const char*)d_in[9], (const char*)d_in[8],
                                                (const char*)d_in[0], (char*)d_ws, (const char*)d_in[2], (const char*)d_in[4],
                                                (const char*)d_in[6], (const char*)d_in[7], (char*)d_out, (char*)d_out);
}
```

```cpp
#include <hip/hip_runtime.h>

typedef unsigned u32;
typedef unsigned long long u64;
typedef unsigned short u16;
typedef u32 v4u __attribute__((ext_vector_type(4)));
typedef u32 v3u __attribute__((ext_vector_type(3)));
typedef u32 v2u __attribute__((ext_vector_type(2)));
typedef float v4f __attribute__((ext_vector_type(4)));
typedef float v2f __attribute__((ext_vector_type(2)));
typedef u32 v8u __attribute__((ext_vector_type(8)));
typedef u32 v16u __attribute__((ext_vector_type(16)));
typedef u32 v32u __attribute__((ext_vector_type(32)));
typedef __bf16 v8bf __attribute__((ext_vector_type(8)));
typedef __bf16 v2bf __attribute__((ext_vector_type(2)));
typedef __amdgpu_buffer_rsrc_t rsrc_t;

#define WA 3
#define WB 3
#define WSW 10
#define QD 8
#define SD 24
#define TM 230
#define TP 515
#define RD 2
#define RP 2
#define R2D 2
#define R2P 0
#define FD 1
#define FP_ 0
#define WD 1
#define WP_ 0
#define PWS 20
#define WTS 20
#define JOINT 0
#define XT 100
#define YT 410
#define FSC "sc1"
#define WSC 16
#define VP 3
#define FS 1
#define FG 8
#define PFI 7
#define P1B 9
#define P2B 9
#define DL0 150
#define DLS 36
#define DLF 99
#define KS 16
#define KR 0x380
#define PAIR 0
#define GA 0
#define GT 800
#define PM 1
#define G0 0
#define TT 0
#define PP 0
#define GNT 2
#define GS 8
#define VR 0
#define KFAKE 0
#define MPO 2097152
#define MM 1
#define MQ 1
#define MPS 0
#define QPS 0
#define SX 0
#define STR_(x) #x
#define STR(x) STR_(x)

constexpr u32 WS_MP = MPO;
constexpr u32 MPB = 43008;
constexpr u32 WS_ST = 196608;
constexpr u32 WS_QP = 1310720;
constexpr u32 WS_WP = 262144;
constexpr u32 WS_XS = 524288;
constexpr u32 XSB = 43008;
constexpr u32 WS_XE = 655360;
constexpr u32 WS_FP = 393216;
constexpr u32 WS_O = 720896;
constexpr u32 WS_END = WS_MP + 2 * MPB;
constexpr u32 OOB = 0x40000000u;
#define SPIN_CAP_V 400000u
constexpr u32 SPIN_CAP = SPIN_CAP_V;

constexpr int L_ML = 0;
constexpr int L_QS = 128;
constexpr int L_OP = L_QS + 1280;
constexpr int L_SS = L_OP + 16384;
constexpr int L_P1 = L_SS + 128;
constexpr int L_P2 = L_P1 + 7 * 2064;
constexpr int L_END = L_P2 + 7 * 2064;

#define RS(p, n) __builtin_amdgcn_make_buffer_rsrc((void*)(p), (short)0, (int)(n), 0x00020000)
#define LD_NT(rs, vo, so) __builtin_amdgcn_raw_buffer_load_b128(rs, (int)(vo), (int)(so), 2)
#define LD_G(rs, vo, so) __builtin_amdgcn_raw_buffer_load_b128(rs, (int)(vo), (int)(so), GNT)
#define LD_C4(rs, vo, so) __builtin_amdgcn_raw_buffer_load_b128(rs, (int)(vo), (int)(so), 16)
#define ST_C4(v, rs, vo) __builtin_amdgcn_raw_buffer_store_b128(v, rs, (int)(vo), 0, 16)
#define ST_C2(v, rs, vo) __builtin_amdgcn_raw_buffer_store_b64(v, rs, (int)(vo), 0, 16)
#define FENCE() asm volatile("" ::: "memory")
#define ARG(v, i) ((char*)(((u64)v[2 * (i) + 1] << 32) | v[2 * (i)]))
#define wo ARG(kb, 0)
#define w1 ARG(kb, 1)
#define w2 ARG(kb, 2)
#define out ARG(kb, 3)
#define EXIT() __builtin_amdgcn_endpgm()
#define KEEP(x) asm volatile("" : "+v"(x))
#define KEEPS(x) asm volatile("" : "+s"(x))
#define KEEP8(x) asm volatile("" : "+v"(x[0]), "+v"(x[1]), "+v"(x[2]), "+v"(x[3]), "+v"(x[4]), "+v"(x[5]), "+v"(x[6]), "+v"(x[7]))
#define ANY8(x) asm volatile("" : "=v"(x[0]), "=v"(x[1]), "=v"(x[2]), "=v"(x[3]), "=v"(x[4]), "=v"(x[5]), "=v"(x[6]), "=v"(x[7]))
__device__ __forceinline__ bool spin_more(u32& n) { asm volatile("" : "+s"(n)); return --n != 0; }
#define SPIN(n) _Pragma("unroll 1") for (u32 sp_ = (n), go_ = 1; go_; go_ = spin_more(sp_))
#define POLL(D, P, ISSUE, CHECK) do { \
    _Pragma("unroll") for (int d_ = 0; d_ + 1 < D; ++d_) { ISSUE(d_); FENCE(); if (P) __builtin_amdgcn_s_sleep(P); } \
    SPIN(SPIN_CAP) { \
        bool ok_ = false; \
        _Pragma("unroll") for (int d_ = 0; d_ < D; ++d_) { \
            ISSUE((d_ + D - 1) % D); \
            FENCE(); \
            CHECK(d_); \
            if (NONE(bad)) { ok_ = true; break; } \
            if (P) __builtin_amdgcn_s_sleep(P); \
        } \
        if (ok_) break; \
    } } while (0)
#define NONE(bad) (__builtin_amdgcn_ballot_w64((bad) != 0u) == 0ull)
#define OFF(c) ((c) ? OOB : 0u)
#define PIN() __builtin_amdgcn_sched_barrier(0)
#define BAR() do { asm volatile("s_waitcnt lgkmcnt(0)\n\ts_barrier" ::: "memory"); } while (0)
#define VMWAIT(n) asm volatile("s_waitcnt vmcnt(%0)" :: "i"(n) : "memory")
#define DWAIT(i) do { if (i < DLF) VMWAIT(win(i) - 1); else asm volatile("s_memrealtime s[60:61]\n\ts_waitcnt lgkmcnt(0)\n\ts_sub_u32 s60, s60, %0\n\ts_cmpk_gt_u32 s60, %2\n\t" \
    "s_cbranch_scc1 .Ldw%=\n\ts_waitcnt vmcnt(%1)\n.Ldw%=:" :: "s"((u32)tz), "i"(win(i) - 1), "i"(DL0 + DLS * (i - 2)) : "s60", "s61", "scc", "memory"); } while (0)

__device__ __forceinline__ float asf(u32 x) { return __builtin_bit_cast(float, x); }
__device__ __forceinline__ u32 asu(float x) { return __builtin_bit_cast(u32, x); }
__device__ __forceinline__ v4f asf4(v4u x) { return __builtin_bit_cast(v4f, x); }
__device__ __forceinline__ float lo16(u32 x) { return asf(x << 16); }
__device__ __forceinline__ float hi16(u32 x) { return asf(x & 0xffff0000u); }
__device__ __forceinline__ u32 pk(float a, float b) { v2bf v = {(__bf16)a, (__bf16)b}; return __builtin_bit_cast(u32, v); }
__device__ __forceinline__ float rb(float a) { return (float)(__bf16)a; }
__device__ __forceinline__ void rb2(float& a, float& b) { const u32 t = pk(a, b); a = lo16(t); b = hi16(t); }
template <int C> __device__ __forceinline__ float dpp(float x) { return asf((u32)__builtin_amdgcn_update_dpp(0, (int)asu(x), C, 0xf, 0xf, true)); }
template <int C> __device__ __forceinline__ u32 dppu(u32 x) { return (u32)__builtin_amdgcn_update_dpp(0, (int)x, C, 0xf, 0xf, true); }
constexpr int QX1 = 0xB1, QX2 = 0x4E, HMIR = 0x141, ROR4 = 0x124, ROR8 = 0x128;
__device__ __forceinline__ float t32_add(float a, float b) { auto s = __builtin_amdgcn_permlane32_swap(asu(a), asu(b), false, false); return asf(s[0]) + asf(s[1]); }
__device__ __forceinline__ float t16_add(float a, float b) { auto s = __builtin_amdgcn_permlane16_swap(asu(a), asu(b), false, false); return asf(s[0]) + asf(s[1]); }
__device__ __forceinline__ float fmx(float a, float b) { return __builtin_fmaxf(a, b); }
__device__ __forceinline__ float x32_max(float x) { auto s = __builtin_amdgcn_permlane32_swap(asu(x), asu(x), false, false); return fmx(asf(s[0]), asf(s[1])); }
__device__ __forceinline__ float rows4(v4f a) { return t16_add(t32_add(a[0], a[2]), t32_add(a[1], a[3])); }
__device__ __forceinline__ float sum_lo3(float x) { x += dpp<QX1>(x); x += dpp<QX2>(x); x += dpp<HMIR>(x); return x; }
__device__ __forceinline__ float max_lo3(float x) { x = fmx(x, dpp<QX1>(x)); x = fmx(x, dpp<QX2>(x)); x = fmx(x, dpp<HMIR>(x)); return x; }
__device__ __forceinline__ float sum_row(float x) { x = sum_lo3(x); return x + dpp<ROR8>(x); }
__device__ __forceinline__ float max_row(float x) { x = max_lo3(x); return fmx(x, dpp<ROR8>(x)); }
__device__ __forceinline__ float sum_nb(float x) { x += dpp<QX1>(x); x += dpp<ROR4>(x); x += dpp<ROR8>(x); return t16_add(x, x); }
__device__ __forceinline__ v4f mfma(v4u a, v4u b, v4f c) {
    return __builtin_amdgcn_mfma_f32_16x16x32_bf16(__builtin_bit_cast(v8bf, a), __builtin_bit_cast(v8bf, b), c, 0, 0, 0);
}
__device__ __forceinline__ float silu(float v) { return v * __builtin_amdgcn_rcpf(1.f + __builtin_amdgcn_exp2f(v * -1.44269504089f)); }
__device__ __forceinline__ float rdl(float v, int lane) { return asf((u32)__builtin_amdgcn_readlane((int)asu(v), lane)); }
constexpr float SC = 0.0625f * 1.44269504089f;

template <int T> __device__ __forceinline__ void tr2(const v4u& r0, const v4u& r1, v4u* o) {
#pragma unroll
    for (int j = 0; j < 8; ++j) o[j][T] = __builtin_amdgcn_perm(r1[j >> 1], r0[j >> 1], (j & 1) ? 0x07060302u : 0x05040100u);
}
__device__ __forceinline__ void tr8(const v4u* r, v4u* o) { tr2<0>(r[0], r[1], o); tr2<1>(r[2], r[3], o); tr2<2>(r[4], r[5], o); tr2<3>(r[6], r[7], o); }
__device__ __forceinline__ v4u seal(v4f a, u32 tag) {
    return v4u{__builtin_amdgcn_perm(asu(a[0]), tag, 0x07060500u), __builtin_amdgcn_perm(asu(a[1]), tag, 0x07060501u),
               __builtin_amdgcn_perm(asu(a[2]), tag, 0x07060502u), __builtin_amdgcn_perm(asu(a[3]), tag, 0x07060503u)};
}
__device__ __forceinline__ v4u tag_bytes(u32 t) { return v4u{t & 255u, (t >> 8) & 255u, (t >> 16) & 255u, t >> 24}; }
__device__ __forceinline__ u32 or4(v4u y) { return y.x | y.y | y.z | y.w; }

#define STAMP(i)
#define STAMPV(i, x)
#define DUMP()

__device__ __forceinline__ void ln_stat(float s1, float s2, float& M, float& r) {
    M = s1 * (1.f / 448);
    const float V = fmaxf(s2 * (1.f / 448) - M * M, 0.f);
    r = __builtin_amdgcn_rcpf(rb(__builtin_amdgcn_sqrtf(rb(rb(V) + 1e-5f))));
    M = rb(M);
}
template <int QB, int JJ> __device__ __forceinline__ u32 bank_mov(u32 old, u32 P) {
    constexpr int C = QB == JJ ? 0xE4 : QB > JJ ? 0x110 + 4 * (QB - JJ) : 0x100 + 4 * (JJ - QB);
    return (u32)__builtin_amdgcn_update_dpp((int)old, (int)P, C, 0xf, 1 << QB, false);
}
template <int QB> __device__ __forceinline__ void bank7(v4u& A, v4u& A2, u32 P0, u32 P1) {
    A[0] = bank_mov<QB, 0>(A[0], P0); A[1] = bank_mov<QB, 0>(A[1], P1); A[2] = bank_mov<QB, 1>(A[2], P0); A[3] = bank_mov<QB, 1>(A[3], P1);
    A2[0] = bank_mov<QB, 2>(A2[0], P0); A2[1] = bank_mov<QB, 2>(A2[1], P1); A2[2] = bank_mov<QB, 3>(A2[2], P1);
}
__device__ __forceinline__ void zero4(v4u& A) { A = v4u{0, 0, 0, 0}; asm volatile("" : "+v"(A)); }
__device__ __forceinline__ float fold(v4f a) {
    return asf((u32)__builtin_amdgcn_update_dpp((int)asu(a[0]), (int)asu(a[2]), ROR8, 0xf, 0xc, false)) +
           asf((u32)__builtin_amdgcn_update_dpp((int)asu(a[3]), (int)asu(a[1]), ROR8, 0xf, 0x3, false));
}
template <int C, int BM> __device__ __forceinline__ u32 qmov(u32 old, u32 P) { return (u32)__builtin_amdgcn_update_dpp((int)old, (int)P, C, 0xf, BM, false); }
__device__ __forceinline__ v4f fold2(const v4f* c) {
    return v4f{c[0][0] + dpp<ROR8>(c[0][1]), c[1][0] + dpp<ROR8>(c[1][1]), c[2][0] + dpp<ROR8>(c[2][1]), c[3][0] + dpp<ROR8>(c[3][1])};
}
__device__ __forceinline__ v4f gemv(u32 P, v4u* A, const v4u* B) {
    const u32 Q = dppu<ROR8>(P);
    A[0][0] = qmov<0x88, 3>(A[0][0], P); A[0][1] = qmov<0xDD, 3>(A[0][1], P); A[1][2] = qmov<0x88, 12>(A[1][2], P); A[1][3] = qmov<0xDD, 12>(A[1][3], P);
    A[0][2] = qmov<0x88, 3>(A[0][2], Q); A[0][3] = qmov<0xDD, 3>(A[0][3], Q); A[1][0] = qmov<0x88, 12>(A[1][0], Q); A[1][1] = qmov<0xDD, 12>(A[1][1], Q);
    v4f c[4];
#pragma unroll
    for (int s = 0; s < 4; ++s) c[s] = mfma(A[0], B[s], v4f{0, 0, 0, 0});
#pragma unroll
    for (int s = 0; s < 4; ++s) c[s] = mfma(A[1], B[4 + s], c[s]);
    return fold2(c);
}
__device__ __forceinline__ v4u seal(v4f a, u32 tag, const v4u& sel) {
    return v4u{__builtin_amdgcn_perm(asu(a[0]), tag, sel[0]), __builtin_amdgcn_perm(asu(a[1]), tag, sel[1]),
               __builtin_amdgcn_perm(asu(a[2]), tag, sel[2]), __builtin_amdgcn_perm(asu(a[3]), tag, sel[3])};
}
#define SEL(x) v4u x = {0x07060500u, 0x07060501u, 0x07060502u, 0x07060503u}; KEEP(x)
__device__ __forceinline__ void until(u64 tz, u32 d) {
    const u32 t0 = (u32)tz;
    while ((u32)__builtin_amdgcn_s_memrealtime() - t0 < d) __builtin_amdgcn_s_sleep(1);
}
constexpr int win(int n) { return n == 2 ? 2 : n < WSW ? WA : WB; }

__global__ void __launch_bounds__(704, 3)
adaln_block_kernel(const char*, const char*, const char*, const char*, const char*, const char*, char*, const char*, const char*,
                   const char*, char*) {
    __shared__ __attribute__((aligned(16))) char lds[L_END];
    const char *xin, *kc, *vc;
    char* ws;
    u32 bid = blockIdx.x, tid, w64, io;
    v8u kb;
    u64 tz;
    asm volatile(R"(
	s_load_dwordx16 s[36:51], s[0:1], 0x0
	s_getpc_b64 s[20:21]
.Ladaln_pc:
	s_lshr_b32 s18, s2, 3
	s_cmp_eq_u32 s18, )" STR(PFI) R"(
	s_cselect_b32 s22, max(.Ladaln_e0-.Ladaln_pc, .Ladaln_e1-.Ladaln_pc), 0
	s_mov_b32 s23, 0x20000
	v_xor_b32 v1, 0x80, v0
	v_lshlrev_b32 v1, 7, v1
	buffer_load_dword v167, v1, s[20:23], 0 offen
	s_memrealtime s[92:93]
	s_load_dwordx8 s[84:91], s[0:1], 0x38
	v_readfirstlane_b32 s17, v0
	v_bfe_u32 v2, v0, 3, 3
	v_and_b32 v3, 7, v0
	s_mov_b32 s22, -1
	s_cmp_gt_u32 s17, 64
	s_cbranch_scc1 .Ladaln_st
	s_setprio 2
	s_and_b32 s18, s2, 3
	s_mulk_i32 s18, 112
	s_lshr_b32 s19, s2, 2
	s_lshl_b32 s19, s19, 7
	v_mad_u32_u24 v2, v2, 14, s18
	v_lshl_add_u32 v3, v3, 4, s19
	s_movk_i32 s52, 0x2000
	s_cmp_eq_u32 s17, 0
	s_cmovk_i32 s52, 5376
	v_mad_u32_u24 v2, v2, s52, v3
	s_add_u32 s53, s52, s52
	s_add_u32 s54, s53, s52
	s_add_u32 s55, s54, s52
	s_add_u32 s56, s55, s52
	s_add_u32 s57, s56, s52
	s_add_u32 s58, s57, s52
	s_add_u32 s59, s58, s52
	s_add_u32 s60, s59, s52
	s_add_u32 s61, s60, s52
	s_add_u32 s62, s61, s52
	s_add_u32 s63, s62, s52
	s_add_u32 s64, s63, s52
	v_and_b32 v4, 12, v0
	v_min_u32 v4, 10, v4
	v_bfe_u32 v5, v0, 4, 2
	v_mad_u32_u24 v4, v5, 28, v4
	v_and_b32 v3, 1, v0
	v_mad_u32_u24 v4, v3, 14, v4
	s_movk_i32 s26, 224
	v_and_b32 v5, 2, v0
	v_mad_u32_u24 v4, v5, s26, v4
	v_add_lshl_u32 v4, v4, s18, 1
	s_movk_i32 s30, 1792
	s_mov_b32 s31, s23
	s_add_u32 s19, s17, s2
	s_cmp_ge_u32 s19, 56
	s_cbranch_scc1 .Ladaln_slow
	s_waitcnt lgkmcnt(0)
	s_load_dword s94, s[48:49], 0x0
	s_mov_b64 s[20:21], s[36:37]
	s_mov_b64 s[28:29], s[40:41]
.Ladaln_go:
	buffer_load_dwordx2 v[110:111], v4, s[28:31], 0 offen
	buffer_load_dwordx4 v[112:115], v2, s[20:23], 0 offen nt
	buffer_load_dwordx4 v[116:119], v2, s[20:23], s52 offen nt
	buffer_load_dwordx4 v[120:123], v2, s[20:23], s53 offen nt
	buffer_load_dwordx4 v[124:127], v2, s[20:23], s54 offen nt
	buffer_load_dwordx4 v[128:131], v2, s[20:23], s55 offen nt
	buffer_load_dwordx4 v[132:135], v2, s[20:23], s56 offen nt
	buffer_load_dwordx4 v[136:139], v2, s[20:23], s57 offen nt
	buffer_load_dwordx4 v[140:143], v2, s[20:23], s58 offen nt
	buffer_load_dwordx4 v[144:147], v2, s[20:23], s59 offen nt
	buffer_load_dwordx4 v[148:151], v2, s[20:23], s60 offen nt
	buffer_load_dwordx4 v[152:155], v2, s[20:23], s61 offen nt
	buffer_load_dwordx4 v[156:159], v2, s[20:23], s62 offen nt
	buffer_load_dwordx4 v[160:163], v2, s[20:23], s63 offen nt
	buffer_load_dwordx4 v[164:167], v2, s[20:23], s64 offen nt
	s_branch .Ladaln_end
.Ladaln_slow:
	v_bfe_u32 v1, v0, 2, 4
	v_mul_u32_u24 v1, 28, v1
	v_mad_u32_u24 v1, v3, 14, v1
	v_mad_u32_u24 v1, v5, s26, v1
	v_lshlrev_b32 v1, 1, v1
	s_cmp_eq_u32 s17, 0
	s_waitcnt lgkmcnt(0)
	s_load_dword s94, s[48:49], 0x0
	s_cselect_b64 s[20:21], s[36:37], s[38:39]
	s_cselect_b64 s[28:29], s[40:41], s[42:43]
	s_cbranch_scc1 .Ladaln_x2
	buffer_load_dwordx4 v[102:105], v1, s[28:31], 0 offen
	buffer_load_dwordx3 v[106:108], v1, s[28:31], 0 offen offset:16
	s_sleep )" STR(QD) R"(
	s_branch .Ladaln_go
.Ladaln_x2:
	s_sub_u32 s19, s2, 112
	s_cmpk_lt_u32 s19, 112
	s_cbranch_scc0 .Ladaln_idle
	v_subrev_u32 v2, 14*128, v2
.Ladaln_xw:
	s_sleep 1
	s_memrealtime s[26:27]
	s_waitcnt lgkmcnt(0)
	s_sub_u32 s26, s26, s92
	s_cmpk_lt_u32 s26, )" STR(XT) R"(
	s_cbranch_scc1 .Ladaln_xw
	s_branch .Ladaln_go
.Ladaln_idle:
	s_sub_u32 s19, s2, 224
	s_cmp_lt_u32 s19, 28
	s_cbranch_scc1 .Ladaln_end
	s_endpgm
.Ladaln_st:
	s_and_b32 s18, s2, 15
	s_lshl_b32 s18, s18, 18
	s_lshr_b32 s19, s2, )" STR(KS) R"(-9
	s_lshl_b32 s19, s19, )" STR(KS) R"(-1
	s_add_u32 s18, s18, s19
	s_bfe_u32 s19, s2, 4+((  )" STR(KS) R"(-13)<<16)
	s_lshl_b32 s19, s19, 9
	s_add_u32 s18, s18, s19
	s_add_u32 s18, s18, s17
	s_lshl_b32 s18, s18, 4
	s_add_u32 s18, s18, 0x2ff400
	s_lshr_b32 s19, s17, 2
	s_xor_b32 s19, s19, s2
	s_bfe_u32 s19, s19, 4+()" STR(VR) R"(<<16)
	v_xor_b32 v3, s19, v3
	v_lshlrev_b32 v2, 4, v2
	v_lshl_add_u32 v2, v3, )" STR(KS) R"(, v2
.if )" STR(KFAKE) R"(
	v_and_b32 v2, 63, v0
	v_lshlrev_b32 v2, 4, v2
.endif
	s_cmpk_eq_u32 s17, 128
	s_waitcnt lgkmcnt(0)
	s_load_dword s94, s[48:49], 0x0
	s_cbranch_scc1 .Ladaln_end
	s_mov_b64 s[20:21], s[44:45]
	s_lshl_b32 s19, s17, 1
	s_and_b32 s19, s19, )" STR(KR) R"(
	s_or_b32 s18, s18, s19
	s_xor_b32 s19, s18, 128
.if )" STR(KFAKE) R"(
	s_add_u32 s19, s18, 1 << )" STR(KS) R"(
.endif
	s_sleep )" STR(SD) R"(
.if )" STR(SX) R"(
	s_cmpk_lt_u32 s2, )" STR(SX) R"(
	s_cbranch_scc1 .Ladaln_sx
	s_sleep 127
	s_sleep 60
.Ladaln_sx:
.endif
	buffer_load_dwordx4 v[112:115], v2, s[20:23], s18 offen nt
	buffer_load_dwordx4 v[116:119], v2, s[20:23], s19 offen nt
.Ladaln_end:
)"  : "={s[42:43]}"(xin), "={s[44:45]}"(kc), "={s[46:47]}"(vc), "={s[48:49]}"(ws), "+{s2}"(bid), "={s17}"(w64), "={s[84:91]}"(kb), "={s[92:93]}"(tz)
    : "{s[0:1]}"(__builtin_amdgcn_kernarg_segment_ptr()), "{v0}"(threadIdx.x)
    : "memory", "scc", "s18", "s19", "s20", "s21", "s22", "s23", "s26", "s27", "s28", "s29", "s30", "s31", "s36", "s37", "s38", "s39", "s40", "s41", "s50", "s51",
      "s52", "s53", "s54", "s55", "s56", "s57", "s58", "s59", "s60", "s61", "s62", "s63", "s64", "s94", "v1", "v2", "v3", "v5", "v102", "v167");
    v4u R[14], xs0;
    v3u xs1;
    v2u in;
    u32 tag;
    asm volatile("" : "={s94}"(tag));
    asm volatile("" : "={v0}"(tid), "={v4}"(io), "={v[110:111]}"(in), "={v[102:105]}"(xs0), "={v[106:108]}"(xs1),
                 "={v[112:115]}"(R[0]), "={v[116:119]}"(R[1]), "={v[120:123]}"(R[2]), "={v[124:127]}"(R[3]), "={v[128:131]}"(R[4]), "={v[132:135]}"(R[5]), "={v[136:139]}"(R[6]),
                 "={v[140:143]}"(R[7]), "={v[144:147]}"(R[8]), "={v[148:151]}"(R[9]), "={v[152:155]}"(R[10]), "={v[156:159]}"(R[11]), "={v[160:163]}"(R[12]), "={v[164:167]}"(R[13]));
    PIN();
    STAMP(1);
    const int w = w64 >> 6;
    const u32 l = tid & 63, idx = bid >> 3, xc = bid & 7;
    const u32 slab = bid & 15, split = bid >> 4;
    const u32 kq = l >> 4, a8 = l & 7, g8 = l >> 3;
    const u32 fu = bid - 224, ru = bid - 112;
    const bool isW = idx < 14, isF = fu < 28;
    const u32 iw = PP && idx >= 14 ? idx - 14 : idx;
    const bool isG = idx < (PP ? 28 : 14);
    const u32 hq = 2 * xc + (iw >= 7), cg = iw >= 7 ? iw - 7 : iw;
#define rsW RS(ws, WS_END)
#define rsw (v4u{(u32)(u64)ws, (u32)((u64)ws >> 32) & 0xffffu, WS_END, 0x20000u})
#define STAMP0()
#define ARGS() do { asm volatile("s_waitcnt lgkmcnt(0) ; args" : "+{s94}"(tag)); \
                    tag = tag * 1664525u + 1013904223u; STAMP0(); } while (0)
#define TILE2(n, i, r0, r1) PIN(); asm volatile("s_waitcnt vmcnt(" #n ") ; tile " r0 " " r1 : "+{v[" r0 "]}"(R[i]), "+{v[" r1 "]}"(R[i + 1]) :: "memory")

    if (__builtin_expect_with_probability(w < 3, 1, 0.55)) {
    u32 tch = 0;
    if (__builtin_expect_with_probability(w < 2, 1, 0.9)) {
        v4u B0[8], B1[8], A[4];
        v4f c[4];
#pragma unroll
        for (int i = 0; i < 4; ++i) zero4(A[i]);
#pragma unroll
        for (int j = 0; j < 8; ++j) { B1[j][3] = 0; KEEP(B1[j]); }
        const u32 pl = OFF(l >= 32) + (2 * a8 + kq) * 16;
        if (__builtin_expect_with_probability(w == 0, 1, 0.55)) {
            if (isF) {
                asm volatile("; tile 102:167");
                ARGS();
                const v4u tb = tag_bytes(tag);
                const u32 xv = __builtin_amdgcn_raw_buffer_load_b32(RS(xin, 1792), (l & 4) * 224 + fu * 32 + (l & 3) * 8 + (kq & 1) * 4, 0, 0);
                until(tz, YT);
                v4u y0, y1;
                SPIN(SPIN_CAP) {
                    y0 = LD_C4(rsW, WS_XS + fu * 1536 + l * 16, 0) ^ tb;
                    y1 = LD_C4(rsW, WS_XS + 1024 + fu * 1536 + (l & 31) * 16, 0) ^ tb;
                    FENCE();
                    if (NONE((or4(y0) | or4(y1)) & 255u)) break;
                    __builtin_amdgcn_s_sleep(2);
                }
                float e[4] = {t16_add(asf(y0.x), asf(y0.z)), t16_add(asf(y0.y), asf(y0.w)), t16_add(asf(y1.x), asf(y1.z)), t16_add(asf(y1.y), asf(y1.w))};
#pragma unroll
                for (int i = 0; i < 4; ++i) e[i] = rb(e[i] + dpp<ROR8>(e[i]));
                u32 cx = pk(e[2] + 1.f, e[3] + 1.f);
                KEEP(cx);
                cx = l < 32 ? cx : xv;
                ST_C4(seal(v4f{e[0], e[1], lo16(cx), hi16(cx)}, tag), rsW, OFF(l & 8) + WS_XE + fu * 512 + kq * 128 + a8 * 16);
                STAMP(4);
                asm volatile("; e end");
                DUMP();
                EXIT();
            }
            u32 po = pl + WS_MP + (bid & 3) * 10752 + (bid >> 2) * 256 + (g8 & 1) * MPB;
            const u32 xslot = WS_XS + (ru >> 2) % 7 * 6144 + (ru >> 2) / 7 * 512 + (ru >= 84 ? XSB - 1536 : 0) + (bid & 3) * 128;
            u32 p2 = OFF(l >= 32) + xslot + (a8 >> 1) * 1536 + (l & 8) * 8 + (l & 1) * 32 + kq * 16;
            asm volatile("" : "+v"(po), "+v"(p2));
            po = bid >= 56 ? p2 : po;
            KEEP(po);
            asm volatile("s_waitcnt vmcnt(14) ; tile 102:111" : "+{v[110:111]}"(in) :: "memory");
            const u32 P0 = pk(silu(lo16(in.x)), silu(hi16(in.x))), P1 = pk(silu(lo16(in.y)), silu(hi16(in.y)));
            bank7<0>(A[0], A[2], P0, P1); bank7<1>(A[1], A[3], P0, P1);
            TILE2(12, 0, "112:115", "116:119"); tr2<0>(R[0], R[1], B0);
            TILE2(10, 2, "120:123", "124:127"); tr2<1>(R[2], R[3], B0);
            ARGS();
            TILE2(8, 4, "128:131", "132:135"); tr2<2>(R[4], R[5], B0);
            TILE2(6, 6, "136:139", "140:143"); tr2<3>(R[6], R[7], B0);
#pragma unroll
            for (int s = 0; s < 4; ++s) c[s] = mfma(A[0], B0[s], v4f{0, 0, 0, 0});
#pragma unroll
            for (int s = 0; s < 4; ++s) c[s] = mfma(A[1], B0[4 + s], c[s]);
            PIN();
            TILE2(4, 8, "144:147", "148:151"); tr2<0>(R[8], R[9], B1);
            TILE2(2, 10, "152:155", "156:159"); tr2<1>(R[10], R[11], B1);
            TILE2(0, 12, "160:163", "164:167");
            STAMP(2);
            tr2<2>(R[12], R[13], B1);
#pragma unroll
            for (int s = 0; s < 4; ++s) c[s] = mfma(A[2], B1[s], c[s]);
#pragma unroll
            for (int s = 0; s < 4; ++s) c[s] = mfma(A[3], B1[4 + s], c[s]);
            ST_C4(seal(v4f{fold(c[0]), fold(c[1]), fold(c[2]), fold(c[3])}, tag), rsW, po);
            STAMP(4);
            DUMP();
            EXIT();
        }
        asm volatile("s_waitcnt vmcnt(14) ; tile 102:111" : "+{v[110:111]}"(in), "+{v[102:105]}"(xs0), "+{v[106:108]}"(xs1) :: "memory");
        float h[4] = {lo16(in.x), hi16(in.x), lo16(in.y), hi16(in.y)};
        {
            float s1 = 0, s2 = 0;
#pragma unroll
            for (int i = 0; i < 7; ++i) {
                const u32 t = i < 4 ? xs0[i & 3] : xs1[i & 3];
                s1 += lo16(t) + hi16(t);
                s2 += lo16(t) * lo16(t) + hi16(t) * hi16(t);
            }
            s1 = sum_nb(s1); s2 = sum_nb(s2);
            float M, r;
            ln_stat(t32_add(s1, s1), t32_add(s2, s2), M, r);
#pragma unroll
            for (int i = 0; i < 4; ++i) h[i] = rb(rb(h[i] - M) * r);
        }
        asm volatile("" : "+v"(h[0]), "+v"(h[1]), "+v"(h[2]), "+v"(h[3]));
        ARGS();
        TILE2(12, 0, "112:115", "116:119"); tr2<0>(R[0], R[1], B0);
        TILE2(10, 2, "120:123", "124:127"); tr2<1>(R[2], R[3], B0);
        TILE2(8, 4, "128:131", "132:135"); tr2<2>(R[4], R[5], B0);
        TILE2(6, 6, "136:139", "140:143"); tr2<3>(R[6], R[7], B0);
        TILE2(4, 8, "144:147", "148:151"); tr2<0>(R[8], R[9], B1);
        TILE2(2, 10, "152:155", "156:159"); tr2<1>(R[10], R[11], B1);
        PIN();
        u32 po = pl + WS_QP + (bid >> 4) * 4096 + (bid & 3) * 1024 + (bid & 12) * 64 + (g8 & 1) * 65536;
        u32 mo = WS_MP + 2 * io + (l & 2) * ((MPB - 1792) / 2);
        asm volatile("" : "+v"(po), "+v"(mo));
        TILE2(0, 12, "160:163", "164:167"); tr2<2>(R[12], R[13], B1);
        KEEP8(B0); KEEP8(B1);
        v4u tr = tag_bytes(__builtin_amdgcn_alignbit(tag, tag, (io & 4) * 4));
        KEEP(tr);
        PIN();
        STAMP(2);
        until(tz, TM);
        v4u y[8];
        {
            v32u z;
            u32 cnt = SPIN_CAP;
            asm volatile(R"(
	s_mov_b64 s[52:53], -1
	s_mov_b64 s[54:55], -1
	s_mov_b64 s[56:57], -1
	s_mov_b64 s[58:59], -1
	s_mov_b64 s[60:61], -1
	s_mov_b64 s[62:63], -1
	s_mov_b64 s[64:65], -1
	s_mov_b64 s[66:67], -1
.Lm_loop:
	s_mov_b64 exec, s[52:53]
	s_cbranch_execz .Lm_i0
	buffer_load_dwordx4 v[112:115], %2, %3, 0 offen sc1
.Lm_i0:
	s_mov_b64 exec, s[54:55]
	s_cbranch_execz .Lm_i1
	buffer_load_dwordx4 v[116:119], %2, %3, %8 offen sc1
.Lm_i1:
	s_mov_b64 exec, s[56:57]
	s_cbranch_execz .Lm_i2
	buffer_load_dwordx4 v[120:123], %2, %3, %9 offen sc1
.Lm_i2:
	s_mov_b64 exec, s[58:59]
	s_cbranch_execz .Lm_i3
	buffer_load_dwordx4 v[124:127], %2, %3, %10 offen sc1
.Lm_i3:
	s_mov_b64 exec, s[60:61]
	s_cbranch_execz .Lm_i4
	buffer_load_dwordx4 v[128:131], %2, %3, 0 offen offset:1792 sc1
.Lm_i4:
	s_mov_b64 exec, s[62:63]
	s_cbranch_execz .Lm_i5
	buffer_load_dwordx4 v[132:135], %2, %3, %8 offen offset:1792 sc1
.Lm_i5:
	s_mov_b64 exec, s[64:65]
	s_cbranch_execz .Lm_i6
	buffer_load_dwordx4 v[136:139], %2, %3, %9 offen offset:1792 sc1
.Lm_i6:
	s_mov_b64 exec, s[66:67]
	s_cbranch_execz .Lm_i7
	buffer_load_dwordx4 v[140:143], %2, %3, %10 offen offset:1792 sc1
.Lm_i7:
	s_waitcnt vmcnt(0)
	s_mov_b64 vcc, 0
	.irp j, 0, 1, 2, 3, 4, 5, 6, 7
	s_mov_b64 exec, s[52+2*\j:53+2*\j]
	s_cbranch_execz .Lm_c\j
	v_xor_b32 v[112+4*\j], %4, v[112+4*\j]
	v_xor_b32 v[113+4*\j], %5, v[113+4*\j]
	v_xor_b32 v[114+4*\j], %6, v[114+4*\j]
	v_xor_b32 v[115+4*\j], %7, v[115+4*\j]
	v_or_b32 v111, v[112+4*\j], v[113+4*\j]
	v_or3_b32 v111, v[114+4*\j], v[115+4*\j], v111
	v_cmp_ne_u32_sdwa s[52+2*\j:53+2*\j], v111, 0 src0_sel:BYTE_0 src1_sel:DWORD
	s_or_b64 vcc, vcc, s[52+2*\j:53+2*\j]
.Lm_c\j:
	.endr
	s_cbranch_vccz .Lm_done
.if )" STR(MPS) R"(
	s_sleep )" STR(MPS) R"(
.endif
	s_sub_u32 %1, %1, 1
	s_cbranch_scc0 .Lm_loop
.Lm_done:
	s_mov_b64 exec, -1
)" : "=&{v[112:143]}"(z), "+s"(cnt) : "v"(mo), "s"(rsw), "v"(tr.x), "v"(tr.y), "v"(tr.z), "v"(tr.w), "s"(10752), "s"(21504), "s"(32256)
   : "memory", "vcc", "scc", "v111", "s52", "s53", "s54", "s55", "s56", "s57", "s58", "s59", "s60", "s61", "s62", "s63", "s64", "s65", "s66", "s67");
#pragma unroll
            for (int i = 0; i < 8; ++i) y[i] = v4u{z[4 * i], z[4 * i + 1], z[4 * i + 2], z[4 * i + 3]};
        }
        STAMP(3);
        const v4f sa = (asf4(y[0]) + asf4(y[1])) + (asf4(y[2]) + asf4(y[3])), ca = (asf4(y[4]) + asf4(y[5])) + (asf4(y[6]) + asf4(y[7]));
#pragma unroll
        for (int i = 0; i < 4; ++i) h[i] = rb(h[i] * rb(1.f + rb(ca[i]))) + rb(sa[i]);
        const u32 P0 = pk(h[0], h[1]), P1 = pk(h[2], h[3]);
        bank7<0>(A[0], A[2], P0, P1); bank7<1>(A[1], A[3], P0, P1);
#pragma unroll
        for (int s = 0; s < 4; ++s) c[s] = mfma(A[0], B0[s], v4f{0, 0, 0, 0});
#pragma unroll
        for (int s = 0; s < 4; ++s) c[s] = mfma(A[2], B1[s], c[s]);
#pragma unroll
        for (int s = 0; s < 4; ++s) c[s] = mfma(A[1], B0[4 + s], c[s]);
#pragma unroll
        for (int s = 0; s < 4; ++s) c[s] = mfma(A[3], B1[4 + s], c[s]);
        ST_C4(seal(v4f{fold(c[0]), fold(c[1]), fold(c[2]), fold(c[3])}, tag), rsW, po);
        STAMP(4);
    } else {
        asm volatile("s_setprio 2 ; tile 102:166");
        ARGS();
        u32 t0 = OFF(!isW) + WS_WP + cg * 16384 + hq * 128;
        KEEPS(t0);
        u32 ta = t0 + l * 4096, tc = OFF(l >= 6) + WS_ST + ((slab * 2 + (l & 1)) * 16 + split) * 128;
        asm volatile("" : "+v"(ta), "+v"(tc));
        const u32 to = l < 4 ? ta : tc;
        __builtin_amdgcn_s_sleep(PWS);
        tch = __builtin_amdgcn_raw_buffer_load_b32(rsW, to, 0, 16);
    }
    const v4u tb = tag_bytes(tag);
    const u32 hd = slab * 2 + w - 1;
    u32 pf = 0, pfo;
    v4u pfr;
    {
        const u32 a = idx - 14, j = (a * 11) >> 5, fp = xc + 8 * j, n = (a - 3 * j) * 288 + l;
        const char* pb = w == 1 ? w1 + fp * 128 : w2 + fp * 57344;
        pfr = v4u{(u32)(u64)pb, (u32)((u64)pb >> 32) & 0xffffu, a < 12 && fp < 28 ? (w == 1 ? 447 * 3584 + 128 : 57344) : 0u, 0x20000u};
        u32 o1 = (n >> 1) * 3584 + (n & 1) * 64, o2 = n * 64;
        asm volatile("" : "+v"(o1), "+v"(o2));
        pfo = w == 1 ? o1 : o2;
    }
    u32 pfs = w == 1 ? 32 * 3584 : 4096;
    KEEP(pfo);
    asm volatile("" : "+s"(pfr), "+s"(pfs));
#define PREF(b) if (P1B == b || P2B == b) { if ((w == 1 ? P1B : P2B) == b) asm volatile( \
        ".rept 5\n\tbuffer_load_dword %0, %1, %2, 0 offen\n\tv_add_u32 %1, %3, %1\n\t.endr ; " #b : "+v"(pf), "+v"(pfo) : "s"(pfr), "s"(pfs) : "memory"); }
    until(tz, TP);
    {
        v4u y[4];
        const u32 qo = WS_QP + hd * 4096 + l * 16;
        {
            v16u z;
            u32 cnt = SPIN_CAP;
            asm volatile(R"(
	s_mov_b64 s[52:53], -1
	s_mov_b64 s[54:55], -1
	s_mov_b64 s[56:57], -1
	s_mov_b64 s[58:59], -1
.Lq_loop:
	s_mov_b64 exec, s[52:53]
	s_cbranch_execz .Lq_i0
	buffer_load_dwordx4 v[40:43], %2, %3, 0 offen offset:0 sc1
.Lq_i0:
	s_mov_b64 exec, s[54:55]
	s_cbranch_execz .Lq_i1
	buffer_load_dwordx4 v[44:47], %2, %3, 0 offen offset:1024 sc1
.Lq_i1:
	s_mov_b64 exec, s[56:57]
	s_cbranch_execz .Lq_i2
	buffer_load_dwordx4 v[48:51], %2, %3, 0 offen offset:2048 sc1
.Lq_i2:
	s_mov_b64 exec, s[58:59]
	s_cbranch_execz .Lq_i3
	buffer_load_dwordx4 v[52:55], %2, %3, 0 offen offset:3072 sc1
.Lq_i3:
	s_waitcnt vmcnt(0)
	s_mov_b64 vcc, 0
	.irp j, 0, 1, 2, 3
	s_mov_b64 exec, s[52+2*\j:53+2*\j]
	s_cbranch_execz .Lq_c\j
	v_xor_b32 v[40+4*\j], %4, v[40+4*\j]
	v_xor_b32 v[41+4*\j], %5, v[41+4*\j]
	v_xor_b32 v[42+4*\j], %6, v[42+4*\j]
	v_xor_b32 v[43+4*\j], %7, v[43+4*\j]
	v_or_b32 v39, v[40+4*\j], v[41+4*\j]
	v_or3_b32 v39, v[42+4*\j], v[43+4*\j], v39
	v_cmp_ne_u32_sdwa s[52+2*\j:53+2*\j], v39, 0 src0_sel:BYTE_0 src1_sel:DWORD
	s_or_b64 vcc, vcc, s[52+2*\j:53+2*\j]
.Lq_c\j:
	.endr
	s_cbranch_vccz .Lq_done
.if )" STR(QPS) R"(
	s_sleep )" STR(QPS) R"(
.endif
	s_sub_u32 %1, %1, 1
	s_cbranch_scc0 .Lq_loop
.Lq_done:
	s_mov_b64 exec, -1
)" : "=&{v[40:55]}"(z), "+s"(cnt) : "v"(qo), "s"(rsw), "s"(tb.x), "s"(tb.y), "s"(tb.z), "s"(tb.w)
   : "memory", "vcc", "scc", "v39", "s52", "s53", "s54", "s55", "s56", "s57", "s58", "s59");
#pragma unroll
            for (int i = 0; i < 4; ++i) y[i] = v4u{z[4 * i], z[4 * i + 1], z[4 * i + 2], z[4 * i + 3]};
        }
        const v4f q = (asf4(y[0]) + asf4(y[1])) + (asf4(y[2]) + asf4(y[3]));
        *(v2u*)(lds + L_QS + (w - 1) * 640 + l * 8) = v2u{pk(q[0], q[1]), pk(q[2], q[3])};
    }
    STAMP(5);
    KEEP(tch);
    PREF(0);
    BAR();
    PREF(1);
    BAR();
    {
        const v2f ml = *(const v2f*)(lds + L_ML + (w - 1) * 64 + a8 * 8);
        const float M = max_lo3(ml.x);
        const float f = __builtin_amdgcn_exp2f((ml.x - M) * SC);
        const float L = sum_lo3(f * ml.y);
        ST_C4((v4u{asu(M), tag, asu(L), tag}), rsW, l ? OOB : WS_ST + (hd * 16 + split) * 128);
        float fs[8];
#pragma unroll
        for (int i = 0; i < 8; ++i) { fs[i] = rdl(f, i); KEEP(fs[i]); }
        u32 ra = L_OP + (w - 1) * 512 + (l >> 5) * 1024 + (l & 31) * 16, oa = WS_O + (hd * 16 + split) * 1024 + l * 16;
        asm volatile("" : "+v"(ra), "+v"(oa));
        PREF(2);
        STAMP(6);
        BAR();
        v4f x[8];
#pragma unroll
        for (int i = 0; i < 8; ++i) x[i] = *(const v4f*)(lds + ra + i * 2048);
        v4f o0 = x[0] * fs[0], o1 = x[1] * fs[1];
#pragma unroll
        for (int i = 2; i < 8; i += 2) { o0 += x[i] * fs[i]; o1 += x[i + 1] * fs[i + 1]; }
        ST_C4(seal(o0 + o1, tag), rsW, oa);
        PREF(3);
        STAMP(7);
    }
    if (isW) asm volatile("s_barrier" ::: "memory");
    if (w == 2 || ru >= 28) { DUMP(); asm volatile("; pf" :: "v"(pf)); EXIT(); }
    const bool hi8 = l & 8;
    float m0, m1, m2;
    u32 xo = (l & 4) * 224 + ru * 32 + (l & 3) * 8 + kq * 2;
    {
        const u32 xv = __builtin_amdgcn_raw_buffer_load_b16(RS(xin, 1792), xo, 0, 0);
        const u32 mo = WS_XS + ru * 1536 + (l & 8) * (XSB / 8) + kq * 128 + a8 * 16;
        v4u y0;
        SPIN(SPIN_CAP) {
            y0 = LD_C4(rsW, mo, 0) ^ tb;
            FENCE();
            if (NONE(or4(y0) & 255u)) break;
        }
        asm volatile("; pf r" :: "v"(pf));
        m1 = rb(rows4(asf4(y0))); m2 = dpp<ROR8>(m1);
        m0 = lo16(xv);
    }
    xo = hi8 ? OOB : xo;
    const rsrc_t rso = RS(out, 1792);
    tch = __builtin_amdgcn_raw_buffer_load_b16(rso, xo, 0, 16);
    STAMP(8);
    {
        const u32 po = WS_WP + ru * 4096 + l * 16;
        v4u y0, y1;
        SPIN(SPIN_CAP) {
            y0 = LD_C4(rsW, po, 0) ^ tb;
            y1 = LD_C4(rsW, po + 1024, 0) ^ tb;
            FENCE();
            if (NONE((or4(y0) | or4(y1)) & 255u)) break;
            __builtin_amdgcn_s_sleep(RP);
        }
        STAMP(9);
        float t = rows4(asf4(y0) + asf4(y1));
        t += dpp<ROR8>(t);
        m0 = rb(m0 + rb(m1 * rb(t)));
        __builtin_amdgcn_raw_buffer_store_b32(tag, rsW, (ru | l) ? OOB : 0, 0, 16);
    }
    KEEP(tch);
    STAMP(10);
    v4f s;
    v32u z0;
    u32 cnt = SPIN_CAP;
    {
        u32 oa = WS_FP + ru * 4096 + l * 16, ob = OFF(l >= 32) + oa;
        v4u tc = l < 32 ? tb : v4u{0, 0, 0, 0};
        asm volatile("" : "+v"(oa), "+v"(ob), "+v"(tc), "+v"(xo), "+v"(m0), "+v"(m2));
        asm volatile(R"(
.macro r2_issue b
	buffer_load_dwordx4 v[\b:\b+3], %3, %5, 0 offen sc1
	buffer_load_dwordx4 v[\b+4:\b+7], %3, %5, 0 offen offset:1024 sc1
	buffer_load_dwordx4 v[\b+8:\b+11], %3, %5, 0 offen offset:2048 sc1
	buffer_load_dwordx4 v[\b+12:\b+15], %4, %5, 0 offen offset:3072 sc1
.endm
.macro r2_one b, n, first, t0, t1, t2, t3
	s_waitcnt vmcnt(\n)
	v_xor_b32 v[\b], \t0, v[\b]
	v_xor_b32 v[\b+1], \t1, v[\b+1]
	v_xor_b32 v[\b+2], \t2, v[\b+2]
	v_xor_b32 v[\b+3], \t3, v[\b+3]
.if \first == 1
	v_or_b32 v36, v[\b], v[\b+1]
.else
	v_or3_b32 v36, v[\b], v[\b+1], v36
.endif
.if \first == 2
	v_pk_add_f32 v[32:33], v[\b-4:\b-3], v[\b:\b+1]
	v_pk_add_f32 v[34:35], v[\b-2:\b-1], v[\b+2:\b+3]
.endif
.if \first == 0
	v_pk_add_f32 v[32:33], v[32:33], v[\b:\b+1]
	v_pk_add_f32 v[34:35], v[34:35], v[\b+2:\b+3]
.endif
	v_or3_b32 v36, v[\b+2], v[\b+3], v36
.endm
.macro r2_check b
	r2_one \b, 4*)" STR(R2D) R"(-1, 1, %6, %7, %8, %9
	r2_one \b+4, 4*)" STR(R2D) R"(-2, 2, %6, %7, %8, %9
	r2_one \b+8, 4*)" STR(R2D) R"(-3, 0, %6, %7, %8, %9
	r2_one \b+12, 4*)" STR(R2D) R"(-4, 0, %10, %11, %12, %13
	v_cmp_ne_u32_sdwa vcc, v36, 0 src0_sel:BYTE_0 src1_sel:DWORD
	s_cbranch_vccz .Lr1_done
.if )" STR(R2P) R"(
	s_sleep )" STR(R2P) R"(
.endif
.endm
	.set r_d, 0
	.rept )" STR(R2D) R"(-1
	r2_issue 40+16*r_d
	s_sleep 3
	.set r_d, r_d+1
	.endr
.Lr1_loop:
	.set r_d, 0
	.rept )" STR(R2D) R"(
	r2_issue 40+16*((r_d+)" STR(R2D) R"(-1)%%)" STR(R2D) R"()
	r2_check 40+16*r_d
	.set r_d, r_d+1
	.endr
	s_sub_u32 %2, %2, 1
	s_cbranch_scc0 .Lr1_loop
.Lr1_done:
)" : "=&{v[32:35]}"(s), "=&{v[40:71]}"(z0), "+s"(cnt)
   : "v"(oa), "v"(ob), "s"(rsw), "s"(tb.x), "s"(tb.y), "s"(tb.z), "s"(tb.w), "v"(tc.x), "v"(tc.y), "v"(tc.z), "v"(tc.w) : "memory", "vcc", "scc", "v36");
    }
    STAMP(11);
    {
        float t = rows4(s);
        t += dpp<ROR8>(t);
        __builtin_amdgcn_raw_buffer_store_b16((u16)pk(m0 + rb(m2 * rb(t)), 0.f), rso, xo, 0, 16);
    }
    STAMP(12);
    asm volatile("s_waitcnt vmcnt(0)\n.Ladaln_e0:" :: "{v[40:71]}"(z0));
    DUMP();
    EXIT();
    }
    {
        asm volatile("; tile 102:111 120:163");
        const int v = w - 3;
        u32 kvb = (slab << 22) + (3 << 20) + ((split & ((1 << (KS - 13)) - 1)) << 13) + ((split >> (KS - 13)) << (KS + 3));
        const u32 vr = (w ^ split) & ((1 << VR) - 1);
        u32 ko = ((a8 ^ vr) << KS) + g8 * 16 + v * 1024, vo = l * 16 + v * 1024;
        asm volatile("" : "+v"(ko), "+v"(vo));
        KEEPS(kvb);
        const u32 kr = (w64 << 1) & KR;
        v4u K[8], V[8];
        const rsrc_t rsK = RS(kc + kvb, (7 << KS) + 8192), rsV = RS(vc + kvb, (7 << KS) + 8192);
#pragma unroll
        for (int i = 2; i < 8; ++i) {
            if (!PAIR) DWAIT(i); else if (!(i & 1)) VMWAIT(PAIR - 1);
            K[i] = KFAKE ? LD_NT(rsK, vo, i << KS) : LD_NT(rsK, ko, kr ^ (i * 128));
            FENCE();
        }
        asm volatile("; tile 102:167" : "+{v[112:115]}"(R[0]), "+{v[116:119]}"(R[1]), "+{v[164:167]}"(R[13]));
        K[0] = R[0]; K[1] = R[1];
        ARGS();
        const char* gp = isG ? wo + (size_t)(hq * 256 + (v & 1) * 64 + (v & 2) * 2 + (v >> 2) * 2) * 896 + cg * 128 : w1 + (size_t)v * 64 * 3584 + fu * 128;
        const u32 fsz = v == 7 || !isF ? 0 : 63 * 3584 + 128;
        const rsrc_t rsG = RS(gp, isG ? 185 * 896 + 128 : fsz), rsG2 = RS(gp, fsz);
        u32 gv0 = (isG ? ((g8 & 4) * 32 + (g8 & 3) * 16) * 896 : ((g8 & 2) * 16 + (g8 & 1) * 8 + (g8 >> 2) * 2) * 3584) + a8 * 16, gv1 = gv0 + (isG ? 896 : 3584);
        u32 gs1 = isG ? 8 * 896 : 4 * 3584, gs2 = 16 * 3584, gs3 = 20 * 3584;
        u32 msel = ((l >> 4) ^ l) & 1 ? 4 : (l >> 1) & 3;
        u32 qa = L_QS + (l & 1) * 640 + (l & 2) * 8 + kq * 32, wa = L_OP + v * 2048 + l * 16;
        u32 ma = (l & 55) ? wa : L_ML + (l + v) * 8;
        u32 qj[4], lk = l ^ ((kr >> 4) & 32);
#pragma unroll
        for (int j = 0; j < 4; ++j) { qj[j] = qa + ((kr ^ (j * 128)) & 0x180); KEEP(qj[j]); }
        asm volatile("" : "+v"(gv0), "+v"(gv1), "+v"(msel), "+v"(lk), "+v"(wa), "+v"(ma));
        asm volatile("" : "+s"(gs1), "+s"(gs2), "+s"(gs3));
#pragma unroll
        for (int i = 0; i < 8; ++i) {
            DWAIT((8 + i));
            V[i] = LD_NT(rsV, vo, (i ^ vr) << KS);
            FENCE();
        }
        v4u G[8], PA[4], BV[8];
        ANY8(BV);
        tr2<0>(V[0], V[1], BV); tr2<1>(V[2], V[3], BV);
        KEEP8(BV);
#define GLD(i) G[i] = LD_G(i < 4 ? rsG : rsG2, (i & 1) ? gv1 : gv0, i < 2 ? 0 : i < 4 ? gs1 : i < 6 ? gs2 : gs3)
#define GL(i) PIN(); _Pragma("unroll") for (int g = 0; g < FG; ++g) if ((g < G0 ? G0 : g) == i) GLD(g); FENCE(); PIN()
        STAMP(2);
        BAR();
        STAMP(3);
        v4f s0 = {0, 0, 0, 0}, s1 = {0, 0, 0, 0};
        v4u Q[4];
#pragma unroll
        for (int j = 0; j < 4; ++j) Q[j] = *(const v4u*)(lds + qj[j]);
        GL(0);
#pragma unroll
        for (int j = 0; j < 4; ++j) {
            s0 = mfma(Q[j], K[j], s0);
            s1 = mfma(Q[j], K[4 + j], s1);
        }
        GL(1);
        const v4f sg = lk < 32 ? s0 : s1;
        const float s = rb(fold(v4f{sg[0], sg[2], sg[1], sg[3]}));
        const float m = x32_max(max_lo3(s));
        GL(2);
        const float p = __builtin_amdgcn_exp2f((s - m) * SC);
        float ls = sum_lo3(p);
        ls = t32_add(ls, ls);
        *(v2f*)(lds + ma) = v2f{m, ls};
        GL(3);
        BAR();
        STAMP(4);
        const u32 pp = pk(p, dpp<QX1>(p));
        const u32 x0 = dppu<0x00>(pp), x1 = dppu<0xAA>(pp), y0 = dppu<HMIR>(x0), y1 = dppu<HMIR>(x1);
        GL(4);
#pragma unroll
        for (int c = 0; c < 4; ++c) PA[c] = msel == c ? (c < 2 ? v4u{x0, x1, y0, y1} : v4u{y0, y1, x0, x1}) : v4u{0, 0, 0, 0};
        asm volatile("" : "+v"(PA[0]), "+v"(PA[1]), "+v"(PA[2]), "+v"(PA[3]));
        GL(5);
        tr2<2>(V[4], V[5], BV);
        KEEP8(BV);
        GL(6);
        STAMPV(5, V[7]);
        tr2<3>(V[6], V[7], BV);
        {
            v4f o0 = {0, 0, 0, 0}, o1 = {0, 0, 0, 0};
#pragma unroll
            for (int c = 0; c < 4; ++c) {
                o0 = mfma(PA[c], BV[(c >> 1) * 4 + (c & 1)], o0);
                o1 = mfma(PA[c], BV[(c >> 1) * 4 + (c & 1) + 2], o1);
            }
            GL(7);
            *(v4f*)(lds + wa) = o0;
            *(v4f*)(lds + wa + 1024) = o1;
        }
        BAR();
        STAMP(6);
        if (!isW && !isF) {
            asm volatile("; a exit");
            DUMP();
            EXIT();
        }
        v4u A[2];
        zero4(A[0]); zero4(A[1]);
        const v4u tb = tag_bytes(tag);
        const u32 gl = OFF(l & 8) + (a8 >> 1) * 4096 + (l & 16) * 4 + (l & 1) * 32 + (l >> 5) * 16;
        if (isW) {
            const u32 sb = ((l & 4) * 4 + hq) * 16 + (l & 32) / 4 + (l & 8) / 2;
            u32 so = WS_ST + (sb + (l & 3)) * 128;
            u32 oo = WS_O + sb * 1024 + v * 128 + (l & 16) * 4 + (l & 3) * 16;
            KEEP(so);
#pragma unroll
            for (int i = FG; i < 4; ++i) G[i] = LD_NT(rsG, (i & 1) ? gv1 : gv0, i < 2 ? 0 : gs1);
            FENCE(); PIN();
            v4u st = LD_C4(rsW, so, 0), y[4], B[4];
            FENCE(); PIN();
#define WPERM() _Pragma("unroll") for (int j = 0; j < 4; ++j) { const u32 sel = (j & 1) ? 0x07060302u : 0x05040100u; \
                B[j] = v4u{__builtin_amdgcn_perm(G[1][j >> 1], G[0][j >> 1], sel), __builtin_amdgcn_perm(G[3][j >> 1], G[2][j >> 1], sel), \
                           __builtin_amdgcn_perm(G[1][2 + (j >> 1)], G[0][2 + (j >> 1)], sel), __builtin_amdgcn_perm(G[3][2 + (j >> 1)], G[2][2 + (j >> 1)], sel)}; } \
            asm volatile("" : "+v"(B[0]), "+v"(B[1]), "+v"(B[2]), "+v"(B[3]))
            if (!PM) { WPERM(); }
            u32 pa = L_P1 + v * 1024 + l * 16, ra = L_P1 + (l & 55) * 16 + (l & 8) * 512, go = gl + WS_WP + cg * 16384 + hq * 128;
            SEL(sel);
            asm volatile("" : "+v"(oo), "+v"(pa), "+v"(ra), "+v"(go));
            PIN();
            SPIN(SPIN_CAP) {
                if (NONE((st.y ^ tag) | (st.w ^ tag))) break;
                st = LD_C4(rsW, so, 0);
                FENCE();
            }
            if (WP_) __builtin_amdgcn_s_sleep(WP_);
#pragma unroll
            for (int j = 0; j < 4; ++j) y[j] = LD_C4(rsW, oo + j * 1024, 0);
            FENCE(); PIN();
            float M = asf(st.x);
            M = fmx(M, dpp<QX1>(M)); M = fmx(M, dpp<QX2>(M)); M = x32_max(fmx(M, dpp<ROR8>(M)));
            float f = __builtin_amdgcn_exp2f((asf(st.x) - M) * SC), L = f * asf(st.z);
            L += dpp<QX1>(L); L += dpp<QX2>(L); L += dpp<ROR8>(L);
            f *= __builtin_amdgcn_rcpf(t32_add(L, L));
            float fj[4] = {dpp<0x00>(f), dpp<0x55>(f), dpp<0xAA>(f), dpp<0xFF>(f)};
            asm volatile("" : "+v"(fj[0]), "+v"(fj[1]), "+v"(fj[2]), "+v"(fj[3]));
            PIN();
            if (PM) { WPERM(); PIN(); }
            STAMP(7);
            SPIN(SPIN_CAP) {
                u32 bad = 0;
#pragma unroll
                for (int j = 0; j < 4; ++j) { y[j] ^= tb; bad |= or4(y[j]); }
                if (NONE(bad & 255u)) break;
#pragma unroll
                for (int j = 0; j < 4; ++j) y[j] = LD_C4(rsW, oo + j * 1024, 0);
                FENCE();
            }
            STAMP(8);
            v4f o = (asf4(y[0]) * fj[0] + asf4(y[1]) * fj[1]) + (asf4(y[2]) * fj[2] + asf4(y[3]) * fj[3]);
            float o0 = t32_add(o[0], o[1]), o1 = t32_add(o[2], o[3]);
            o0 += dpp<ROR8>(o0); o1 += dpp<ROR8>(o1);
            const u32 P = pk(o0, o1);
            A[0][0] = qmov<0x88, 3>(A[0][0], P); A[0][1] = qmov<0xDD, 3>(A[0][1], P);
            A[0][2] = qmov<0x88, 12>(A[0][2], P); A[0][3] = qmov<0xDD, 12>(A[0][3], P);
            v4f c[4];
#pragma unroll
            for (int j = 0; j < 4; ++j) c[j] = mfma(A[0], B[j], v4f{0, 0, 0, 0});
            *(v4f*)(lds + pa) = fold2(c);
            BAR();
            if (v != 0) { asm volatile("; w exit"); DUMP(); EXIT(); }
            v4f x[4];
#pragma unroll
            for (int i = 0; i < 4; ++i) x[i] = *(const v4f*)(lds + ra + i * 1024);
            v4f t = (x[0] + x[1]) + (x[2] + x[3]);
#pragma unroll
            for (int i = 0; i < 4; ++i) t[i] += dpp<ROR8>(t[i]);
            __builtin_amdgcn_raw_buffer_store_b128(seal(t, tag, sel), rsW, go, 0, WSC);
            STAMP(10);
            asm volatile("; w end");
            DUMP();
            EXIT();
        }
        if (v == 7) {
            *(v2f*)(lds + L_SS + 56 + (l & 1) * 64) = v2f{0, 0};
            asm volatile("s_waitcnt lgkmcnt(0) ; f exit" ::: "memory");
            DUMP(); EXIT();
        }
        const rsrc_t rs2 = RS(w2 + (size_t)fu * 57344 + v * 128, 63 * 896 + 128);
        const u32 v2 = g8 * 7168 + a8 * 16;
        u32 po = WS_WP + (v * 4 + (g8 & 3)) * 4096 + (l >> 5) * 128 + a8 * 16;
        KEEP(po);
        __builtin_amdgcn_s_sleep(FS);
#pragma unroll
        for (int i = FG; i < 8; ++i) G[i] = LD_NT(rsG2, (i & 1) ? gv1 : gv0, i < 2 ? 0 : i < 4 ? gs1 : i < 6 ? gs2 : gs3);
        const u32 yo = WS_XE + (v * 4 + (g8 & 3)) * 512 + (l >> 5) * 128 + a8 * 16;
        v4u ya = LD_C4(rsW, yo, 0), yb = LD_C4(rsW, yo + 256, 0), B1[8];
        FENCE(); PIN();
        u32 pa = v * 2064 + (l & 16) * 24 + (2 * a8 + (l >> 5)) * 16 + (l & 8) * 128;
        u32 ra = (l & 4) * 96 + kq * 64 + (l & 2) * 16 + (l & 8) * 2 + (l & 1) * 8;
        u32 sa = L_SS - L_OP + (l & 4) * 16 + v * 8 + (l >> 5) * 4, sr = L_SS + (l & 4) * 16 + (l & 8) * 4 + (l & 3) * 8;
        asm volatile("" : "+v"(pa), "+v"(sa));
        sa = (l & 27) ? pa : sa;
        u32 go = gl + WS_FP + v * 16384 + fu * 128;
        SEL(sel);
        asm volatile("" : "+v"(pa), "+v"(ra), "+v"(sa), "+v"(sr), "+v"(go));
        ANY8(B1);
#define VL(n) KEEP8(B1); PIN(); if (n == VP || n == VP + 1) { _Pragma("unroll") for (int i = 0; i < 4; ++i) V[(n - VP) * 4 + i] = LD_NT(rs2, v2 + i * 896, (n - VP) * 3584); } FENCE(); PIN()
        VL(0); tr2<0>(G[0], G[1], B1);
        VL(1); tr2<1>(G[2], G[3], B1);
        VL(2); tr2<2>(G[4], G[5], B1);
        VL(3); tr2<3>(G[6], G[7], B1);
        VL(4);
        STAMP(12);
        ya ^= tb; yb ^= tb;
        if (!NONE((or4(ya) | or4(yb)) & 255u)) SPIN(SPIN_CAP) {
            ya = LD_C4(rsW, yo, 0) ^ tb;
            yb = LD_C4(rsW, yo + 256, 0) ^ tb;
            FENCE();
            if (NONE((or4(ya) | or4(yb)) & 255u)) break;
        }
        *(v4f*)(lds + L_P1 + pa) = gemv(__builtin_amdgcn_perm(ya.w, ya.z, 0x07060302u), A, B1);
        VL(5);
        *(v4f*)(lds + L_P2 + pa) = gemv(__builtin_amdgcn_perm(yb.y, yb.x, 0x07060302u), A, B1);
        float e[6] = {asf(ya.x), asf(ya.y), asf(ya.z), asf(ya.w), asf(yb.z), asf(yb.w)};
        STAMP(11);
        BAR();
        v2f c1, sh1;
        v32u z;
        {
            v2f x[7], y[7];
#pragma unroll
            for (int i = 0; i < 7; ++i) { x[i] = *(const v2f*)(lds + L_P1 + ra + i * 2064); y[i] = *(const v2f*)(lds + L_P2 + ra + i * 2064); }
            KEEP8(V);
            asm volatile(R"(
	.irp j, 0, 1, 2, 3, 4, 5, 6, 7
	buffer_load_dwordx4 v[40+4*\j:43+4*\j], %1, %2, 0 offen offset:256*\j )" FSC R"(
	.endr
)" : "=&{v[40:71]}"(z) : "v"(po), "s"(rsw) : "memory");
            PIN();
            STAMP(7);
            c1 = ((x[0] + x[1]) + (x[2] + x[3])) + ((x[4] + x[5]) + x[6]);
            sh1 = ((y[0] + y[1]) + (y[2] + y[3])) + ((y[4] + y[5]) + y[6]);
            c1 *= 1.f / 448;
            asm volatile("" : "+v"(c1), "+v"(sh1));
        }
        tr8(V, BV);
        KEEP8(BV);
        PIN();
        u32 cnt = SPIN_CAP;
        asm volatile(R"(
	.irp j, 0, 1, 2, 3, 4, 5, 6, 7
	s_mov_b64 s[52+2*\j:53+2*\j], -1
	.endr
	s_branch .Lf_wait
.Lf_loop:
	.irp j, 0, 1, 2, 3, 4, 5, 6, 7
	s_mov_b64 exec, s[52+2*\j:53+2*\j]
	s_cbranch_execz .Lf_i\j
	buffer_load_dwordx4 v[40+4*\j:43+4*\j], %2, %3, 0 offen offset:256*\j )" FSC R"(
.Lf_i\j:
	.endr
.Lf_wait:
	s_waitcnt vmcnt(0)
	s_mov_b64 vcc, 0
	.irp j, 0, 1, 2, 3, 4, 5, 6, 7
	s_mov_b64 exec, s[52+2*\j:53+2*\j]
	s_cbranch_execz .Lf_c\j
	v_xor_b32 v[40+4*\j], %4, v[40+4*\j]
	v_xor_b32 v[41+4*\j], %5, v[41+4*\j]
	v_xor_b32 v[42+4*\j], %6, v[42+4*\j]
	v_xor_b32 v[43+4*\j], %7, v[43+4*\j]
	v_or_b32 v36, v[40+4*\j], v[41+4*\j]
	v_or3_b32 v36, v[42+4*\j], v[43+4*\j], v36
	v_cmp_ne_u32_sdwa s[52+2*\j:53+2*\j], v36, 0 src0_sel:BYTE_0 src1_sel:DWORD
	s_or_b64 vcc, vcc, s[52+2*\j:53+2*\j]
.Lf_c\j:
	.endr
	s_cbranch_vccz .Lf_done
.if )" STR(FP_) R"(
	s_sleep )" STR(FP_) R"(
.endif
	s_sub_u32 %1, %1, 1
	s_cbranch_scc0 .Lf_loop
.Lf_done:
	s_mov_b64 exec, -1
)" : "+{v[40:71]}"(z), "+s"(cnt) : "v"(po), "s"(rsw), "s"(tb.x), "s"(tb.y), "s"(tb.z), "s"(tb.w)
   : "memory", "vcc", "scc", "v36", "s52", "s53", "s54", "s55", "s56", "s57", "s58", "s59", "s60", "s61", "s62", "s63", "s64", "s65", "s66", "s67");
        STAMP(8);
        v4f zz[8];
#pragma unroll
        for (int i = 0; i < 8; ++i) zz[i] = v4f{asf(z[4 * i]), asf(z[4 * i + 1]), asf(z[4 * i + 2]), asf(z[4 * i + 3])};
        const v4f t4 = ((zz[0] + zz[1]) + (zz[2] + zz[3])) + ((zz[4] + zz[5]) + (zz[6] + zz[7]));
        float a0 = t32_add(t4[0], t4[2]), a1 = t32_add(t4[1], t4[3]);
        rb2(a0, a1);
        a0 *= e[0]; a1 *= e[1];
        rb2(a0, a1);
        u32 xp = pk(a0 + e[4], a1 + e[5]);
        KEEP(xp);
        a0 = lo16(xp); a1 = hi16(xp);
        const v4f u4 = gemv(pk(a0 * e[2], a1 * e[3]), A, B1);
        float u = t32_add(a0 + a1, a0 * a0 + a1 * a1);
        u += dpp<QX1>(u); u += dpp<QX2>(u); u += dpp<ROR8>(u);
        *(float*)(lds + L_OP + sa) = t16_add(u, u);
        *(v4f*)(lds + L_OP + pa) = u4;
        BAR();
        STAMP(9);
        v2f ss = *(const v2f*)(lds + sr), x[7];
#pragma unroll
        for (int i = 0; i < 7; ++i) x[i] = *(const v2f*)(lds + L_OP + ra + i * 2064);
        ss.x += dpp<QX1>(ss.x); ss.y += dpp<QX1>(ss.y);
        ss.x += dpp<QX2>(ss.x); ss.y += dpp<QX2>(ss.y);
        ss.x += dpp<ROR8>(ss.x); ss.y += dpp<ROR8>(ss.y);
        const float M = ss.x * (1.f / 448), r = __builtin_amdgcn_rsqf(fmaxf(ss.y * (1.f / 448) - M * M, 0.f) + 1e-5f);
        const v2f t = (((x[0] + x[1]) + (x[2] + x[3])) + ((x[4] + x[5]) + (x[6] - c1 * ss.x))) * r + sh1;
        ST_C4(seal(gemv(pk(silu(t.x), silu(t.y)), A, BV), tag, sel), rsW, go);
        STAMP(10);
        asm volatile(".Ladaln_e1:");
        DUMP();
        EXIT();
    }

}

extern "C" void kernel_launch(void* const* d_in, const int* in_sizes, int n_in, void* d_out, int out_size, void* d_ws, size_t ws_size,
                              hipStream_t stream) {
    adaln_block_kernel<<<256, 704, 0, stream>>>((const char*)d_in[9], (const char*)d_in[3], (const char*)d_in[8], (const char*)d_in[0],
                                                (const char*)d_in[1], (const char*)d_in[2], (char*)d_ws, (const char*)d_in[4],
                                                (const char*)d_in[6], (const char*)d_in[7], (char*)d_out);
}
```
